```python
import torch

def _softmax(x, axis=-1):
    return torch.softmax(x, dim=axis)
def _gelu(x):
    return torch.nn.functional.gelu(x, approximate="tanh")
def _silu(x):
    return torch.nn.functional.silu(x)
def _sigmoid(x):
    return torch.sigmoid(x)
def _layernorm(x, axis=-1, eps=1e-5):
    m = x.mean(dim=axis, keepdim=True)
    v = x.var(dim=axis, keepdim=True, unbiased=False)
    return (x - m) / torch.sqrt(v + eps)
def _scatter_add(tgt, idx, src, axis=0):
    return tgt.clone().index_add_(axis, idx, src)
def _segment_sum(src, seg, n):
    out = torch.zeros((n,) + src.shape[1:], dtype=src.dtype)
    return out.index_add_(0, seg, src)
def _pad(x, pw):
    flat = tuple(v for pair in reversed(pw) for v in pair)
    return torch.nn.functional.pad(x, flat)

kernel_name = 'standalone__sliding_block__prompt'

def setup_inputs():

    _w = (lambda a: (lambda t: t.to(torch.bfloat16) if t.is_floating_point() else t)(torch.as_tensor(a)))
    B, S, D = 1, 2048, 1024
    return {
        "x": _w((lambda *s: torch.randn(*s, dtype=torch.bfloat16) * 0.2)(B, S, D)),
        "wqkv": _w((lambda *s: torch.randn(*s, dtype=torch.bfloat16) * 0.2)(D, 3 * D)),
        "wo": _w((lambda *s: torch.randn(*s, dtype=torch.bfloat16) * 0.05)(D, D)),
    }

def reference(x, wqkv, wo):
    B, S, D = x.shape
    H = 32; Dh = D // H; W = 256
    qkv = (lambda x, *d: x.permute(*d))((x @ wqkv).reshape(B, S, 3, H, Dh), 2, 0, 3, 1, 4)
    q, k, v = qkv[0], qkv[1], qkv[2]
    scores = torch.einsum("bhqd,bhkd->bhqk", q, k) * (Dh ** -0.5)
    idx = torch.arange(S)
    mask = (idx[:, None] - idx[None, :] >= 0) & (idx[:, None] - idx[None, :] < W)
    scores = torch.where(mask[None, None], scores, -1e9)
    p = _softmax(scores, axis=-1)
    o = torch.einsum("bhqk,bhkd->bhqd", p, v)
    return (lambda x, *d: x.permute(*d))(o, 0, 2, 1, 3).reshape(B, S, D) @ wo
```

```cpp
#include <hip/hip_runtime.h>
#include <hip/hip_bf16.h>


typedef short bf16x8 __attribute__((ext_vector_type(8)));
typedef short s16x4 __attribute__((ext_vector_type(4)));
typedef float f32x2 __attribute__((ext_vector_type(2)));
typedef float f32x4 __attribute__((ext_vector_type(4)));
typedef float f32x16 __attribute__((ext_vector_type(16)));
typedef unsigned u32x4 __attribute__((ext_vector_type(4)));
typedef __bf16 bf2_t __attribute__((ext_vector_type(2)));
#define LAS __attribute__((address_space(3)))
#define GAS __attribute__((address_space(1)))
#define DI __device__ __forceinline__
#define MFMA(a, b, c) __builtin_amdgcn_mfma_f32_32x32x16_bf16((a), (b), (c), 0, 0, 0)
#define SBAR() __builtin_amdgcn_sched_barrier(0)
#define BAR() __builtin_amdgcn_s_barrier()
#define WAIT_V(n) asm volatile("s_waitcnt vmcnt(%0)" ::"n"(n) : "memory")
#define WAIT_L0() asm volatile("s_waitcnt lgkmcnt(0)" ::: "memory")

constexpr int SEQ = 2048, DM = 1024, NH = 32;
constexpr int NSTG = 4, STG = 40960, XSZ = 16384;
constexpr size_t WS_Q = 0, WS_KV = 4u << 20, WS_O = 12u << 20;
constexpr float QSCALE = 0.17677669529663687f * 1.4426950408889634f;

DI unsigned pk2(float a, float b) { f32x2 f = {a, b}; return __builtin_bit_cast(unsigned, __builtin_convertvector(f, bf2_t)); }
DI u32x4 pack8(const f32x16& x, int s, float c) {
  u32x4 p;
  p[0] = pk2(x[8 * s] * c, x[8 * s + 1] * c); p[1] = pk2(x[8 * s + 2] * c, x[8 * s + 3] * c);
  p[2] = pk2(x[8 * s + 4] * c, x[8 * s + 5] * c); p[3] = pk2(x[8 * s + 6] * c, x[8 * s + 7] * c);
  return p;
}
DI u32x4 pack8(const f32x16& x, int s) {
  u32x4 p;
  p[0] = pk2(x[8 * s], x[8 * s + 1]); p[1] = pk2(x[8 * s + 2], x[8 * s + 3]);
  p[2] = pk2(x[8 * s + 4], x[8 * s + 5]); p[3] = pk2(x[8 * s + 6], x[8 * s + 7]);
  return p;
}
DI bf16x8 lds128(LAS const char* p) { return *(LAS const bf16x8*)p; }
DI s16x4 ldstr(LAS const char* p) { return __builtin_amdgcn_ds_read_tr16_b64_v4i16((LAS s16x4*)p); }
DI bf16x8 cat(s16x4 a, s16x4 b) { return __builtin_shufflevector(a, b, 0, 1, 2, 3, 4, 5, 6, 7); }
DI void glds(__amdgpu_buffer_rsrc_t r, LAS char* dst, int voff, int soff) {
  __builtin_amdgcn_raw_ptr_buffer_load_lds(r, (LAS void*)dst, 16, voff, soff, 0, 0);
}

__global__ __launch_bounds__(512, 2) void k_qkv(const char* __restrict__ x, const char* __restrict__ w, char* __restrict__ ws) {
  extern __shared__ __attribute__((aligned(16))) char lds_[];
  LAS char* L = (LAS char*)lds_;
  const int bid = blockIdx.x, xcd = bid & 7, idx = bid >> 3;
  const int m = (xcd & 1) * 8 + (idx & 7), hp = (xcd >> 1) * 4 + (idx >> 3);
  const int lane = threadIdx.x & 63, wid = __builtin_amdgcn_readfirstlane(threadIdx.x >> 6);
  if (wid >= 4) {
    const int lw = wid - 4;
    const __amdgpu_buffer_rsrc_t rx = __builtin_amdgcn_make_buffer_rsrc((void*)x, 0, SEQ * DM * 2, 0x00020000);
    const __amdgpu_buffer_rsrc_t rw = __builtin_amdgcn_make_buffer_rsrc((void*)w, 0, DM * 3 * DM * 2, 0x00020000);
    const int vx0 = (m * 128 + 32 * lw + (lane >> 3)) * 2048 + (((lane & 7) ^ (lane >> 4)) << 4), vx1 = vx0 ^ 64;
    const int vw = (16 * lw + (lane >> 3)) * 6144 + hp * 128 + (((lane & 7) ^ (((lane >> 4) & 1) << 2)) << 4);
    auto issue = [&](int kt) {
      LAS char* sb = L + (kt % NSTG) * STG;
      const int sx = kt * 128, sw = kt * 64 * 6144;
#pragma unroll
      for (int i = 0; i < 4; ++i) glds(rx, sb + lw * 4096 + i * 1024, (i & 1) ? vx1 : vx0, sx + i * 16384);
#pragma unroll
      for (int p = 0; p < 3; ++p)
#pragma unroll
        for (int i = 0; i < 2; ++i) glds(rw, sb + XSZ + p * 8192 + lw * 2048 + i * 1024, vw, sw + p * 2048 + i * 8 * 6144);
    };
#pragma unroll
    for (int kt = 0; kt < NSTG; ++kt) issue(kt);
    WAIT_V(10 * (NSTG - 1)); BAR();
    for (int kt = 0; kt < 16 - NSTG; ++kt) { WAIT_V(10 * (NSTG - 2)); BAR(); issue(kt + NSTG); }
#pragma unroll
    for (int kt = 16 - NSTG; kt < 15; ++kt) { WAIT_V(10 * (14 - kt)); BAR(); }
    return;
  }
  const int wm = wid & 1, wn = wid >> 1, hi = lane >> 5;
  const int ax = (64 * wm + (lane & 31)) * 128 + ((hi ^ ((lane >> 1) & 7)) << 4);
  const int q4 = (lane & 15) >> 2, pp = lane & 3, blk = (lane >> 4) & 1;
  const int aw = XSZ + (8 * hi + q4) * 128 + (((4 * wn + 2 * blk + (pp >> 1)) ^ (((q4 >> 1) & 1) << 2)) << 4) + 8 * (pp & 1);
  f32x16 aq[2], ak[2], av[2];
#pragma unroll
  for (int i = 0; i < 2; ++i) { aq[i] = f32x16{}; ak[i] = f32x16{}; av[i] = f32x16{}; }
  bf16x8 xf[2][2], wf[2][3];
#define LOADF(b, sb, s)                                                                                       \
  do {                                                                                                        \
    LAS const char* px = L + (((sb) + ax) ^ ((s) << 5));                                                      \
    LAS const char* pw = L + (sb) + aw + (s) * 2048;                                                          \
    xf[b][0] = lds128(px); wf[b][0] = cat(ldstr(pw), ldstr(pw + 512));                                        \
    wf[b][1] = cat(ldstr(pw + 8192), ldstr(pw + 8192 + 512)); xf[b][1] = lds128(px + 4096);                   \
    wf[b][2] = cat(ldstr(pw + 16384), ldstr(pw + 16384 + 512));                                               \
  } while (0)
#define MMA(b)                                                                                                \
  do {                                                                                                        \
    aq[0] = MFMA(wf[b][0], xf[b][0], aq[0]); ak[0] = MFMA(wf[b][1], xf[b][0], ak[0]);                         \
    aq[1] = MFMA(wf[b][0], xf[b][1], aq[1]); ak[1] = MFMA(wf[b][1], xf[b][1], ak[1]);                         \
    av[0] = MFMA(xf[b][0], wf[b][2], av[0]); av[1] = MFMA(xf[b][1], wf[b][2], av[1]);                         \
  } while (0)
  BAR();
  LOADF(0, 0, 0);
  for (int kt = 0; kt < 16; ++kt) {
    const int sb = (kt % NSTG) * STG, sn = ((kt + 1) % NSTG) * STG;
    LOADF(1, sb, 1); SBAR(); MMA(0); SBAR();
    LOADF(0, sb, 2); SBAR(); MMA(1); SBAR();
    LOADF(1, sb, 3); SBAR(); MMA(0); SBAR();
    if (kt < 15) { WAIT_L0(); BAR(); LOADF(0, sn, 0); }
    SBAR(); MMA(1); SBAR();
  }
#undef LOADF
#undef MMA
  const int h = 2 * hp + wn;
#pragma unroll
  for (int tt = 0; tt < 2; ++tt) {
    const int tile = 4 * m + 2 * wm + tt;
    char* qd = ws + WS_Q + (size_t)(h * 64 + tile) * 2048 + lane * 16;
    char* kd = ws + WS_KV + (size_t)(h * 64 + tile) * 4096 + lane * 16;
#pragma unroll
    for (int s = 0; s < 2; ++s) {
      *(u32x4*)(qd + s * 1024) = pack8(aq[tt], s, QSCALE);
      *(u32x4*)(kd + s * 1024) = pack8(ak[tt], s);
      *(u32x4*)(kd + 2048 + s * 1024) = pack8(av[tt], s);
    }
  }
}

#define MX3(a, b, c) __builtin_fmaxf(__builtin_fmaxf((a), (b)), (c))
DI float max16(const f32x16& p, float m) {
  float a = MX3(p[0], p[1], m), b = MX3(p[2], p[3], p[4]);
  a = MX3(a, p[5], p[6]); b = MX3(b, p[7], p[8]); a = MX3(a, p[9], p[10]); b = MX3(b, p[11], p[12]);
  a = MX3(a, p[13], p[14]); return MX3(a, b, p[15]);
}
__global__ __launch_bounds__(512, 2) void k_attn(char* __restrict__ ws) {
  extern __shared__ __attribute__((aligned(16))) char lds_[];
  LAS char* L = (LAS char*)lds_;
  const int bid = blockIdx.x, xcd = bid & 7, idx = bid >> 3;
  const int m = (xcd & 1) * 8 + (idx & 7), hp = (xcd >> 1) * 4 + (idx >> 3);
  const int lane = threadIdx.x & 63, wid = __builtin_amdgcn_readfirstlane(threadIdx.x >> 6);
  const int hl = wid >> 2, tl = wid & 3, h = 2 * hp + hl, t = 4 * m + tl, hi = lane >> 5;
  {
    const __amdgpu_buffer_rsrc_t rk = __builtin_amdgcn_make_buffer_rsrc((void*)(ws + WS_KV), 0, 8u << 20, 0x00020000);
#pragma unroll
    for (int i = 0; i < 3; ++i) {
      const int j = tl * 3 + i, g = 4 * m - 8 + j;
      if (g >= 0) {
#pragma unroll
        for (int c = 0; c < 4; ++c) glds(rk, L + hl * 49152 + j * 4096 + c * 1024, lane * 16, (h * 64 + g) * 4096 + c * 1024);
      }
    }
  }
  const char* qs = ws + WS_Q + (size_t)(h * 64 + t) * 2048 + lane * 16;
  const bf16x8 q0 = *(const bf16x8*)qs, q1 = *(const bf16x8*)(qs + 1024);
  WAIT_V(0); BAR();
  LAS const char* kb = L + hl * 49152 + tl * 4096 + lane * 16;
  const f32x16 zero = f32x16{};
  f32x16 s[8];
  {
    f32x16 sd = MFMA(lds128(kb + 8 * 4096), q0, zero); sd = MFMA(lds128(kb + 8 * 4096 + 1024), q1, sd);
    f32x16 sf;
    if (t >= 8) { sf = MFMA(lds128(kb), q0, zero); sf = MFMA(lds128(kb + 1024), q1, sf); }
    else { for (int r = 0; r < 16; ++r) sf[r] = -INFINITY; }
#pragma unroll
    for (int r = 0; r < 16; ++r) s[0][r] = ((r & 3) + 8 * (r >> 2) + 4 * hi <= (lane & 31)) ? sd[r] : sf[r];
  }
#pragma unroll
  for (int j = 1; j < 8; ++j) {
    if (t - 8 + j >= 0) { s[j] = MFMA(lds128(kb + j * 4096), q0, zero); s[j] = MFMA(lds128(kb + j * 4096 + 1024), q1, s[j]); }
    else { for (int r = 0; r < 16; ++r) s[j][r] = -INFINITY; }
  }
  float mx = -INFINITY;
#pragma unroll
  for (int j = 0; j < 8; ++j) mx = max16(s[j], mx);
  { auto rr = __builtin_amdgcn_permlane32_swap(__float_as_uint(mx), __float_as_uint(mx), false, false);
    mx = __builtin_fmaxf(__uint_as_float(rr[0]), __uint_as_float(rr[1])); }
  float l = 0.f;
  f32x16 o = zero;
#pragma unroll
  for (int j = 0; j < 8; ++j) {
#pragma unroll
    for (int r = 0; r < 16; ++r) { s[j][r] = __builtin_amdgcn_exp2f(s[j][r] - mx); l += s[j][r]; }
    const u32x4 p0 = pack8(s[j], 0), p1 = pack8(s[j], 1);
    if (j == 0) {
      u32x4 d0, d1;
#pragma unroll
      for (int i = 0; i < 4; ++i) {
        const int k0 = ((2 * i) & 3) + 8 * ((2 * i) >> 2) + 4 * hi, ql = lane & 31;
        const unsigned m0 = (k0 <= ql ? 0xffffu : 0u) | (k0 + 1 <= ql ? 0xffff0000u : 0u);
        const unsigned m1 = (k0 + 16 <= ql ? 0xffffu : 0u) | (k0 + 17 <= ql ? 0xffff0000u : 0u);
        d0[i] = p0[i] & m0; d1[i] = p1[i] & m1;
      }
      o = MFMA(lds128(kb + 8 * 4096 + 2048), __builtin_bit_cast(bf16x8, d0), o);
      o = MFMA(lds128(kb + 8 * 4096 + 3072), __builtin_bit_cast(bf16x8, d1), o);
      if (t >= 8) {
        o = MFMA(lds128(kb + 2048), __builtin_bit_cast(bf16x8, p0 ^ d0), o);
        o = MFMA(lds128(kb + 3072), __builtin_bit_cast(bf16x8, p1 ^ d1), o);
      }
    } else if (t - 8 + j >= 0) {
      o = MFMA(lds128(kb + j * 4096 + 2048), __builtin_bit_cast(bf16x8, p0), o);
      o = MFMA(lds128(kb + j * 4096 + 3072), __builtin_bit_cast(bf16x8, p1), o);
    }
  }
  { auto rr = __builtin_amdgcn_permlane32_swap(__float_as_uint(l), __float_as_uint(l), false, false);
    l = __uint_as_float(rr[0]) + __uint_as_float(rr[1]); }
  const float rl = 1.0f / l;
  char* od = ws + WS_O + (size_t)(t * 32 + h) * 2048 + lane * 16;
  *(u32x4*)od = pack8(o, 0, rl);
  *(u32x4*)(od + 1024) = pack8(o, 1, rl);
}

__global__ __launch_bounds__(512, 2) void k_out(const char* __restrict__ ws, const char* __restrict__ wo, char* __restrict__ out) {
  extern __shared__ __attribute__((aligned(16))) char lds_[];
  LAS char* L = (LAS char*)lds_;
  const int bid = blockIdx.x, xcd = bid & 7, idx = bid >> 3;
  const int m = (xcd & 3) * 4 + (idx & 3), nb = (xcd >> 2) * 8 + (idx >> 2);
  const int lane = threadIdx.x & 63, wid = __builtin_amdgcn_readfirstlane(threadIdx.x >> 6);
  const int tl = wid & 3, kh = wid >> 2, hi = lane >> 5;
  {
    const __amdgpu_buffer_rsrc_t rw = __builtin_amdgcn_make_buffer_rsrc((void*)wo, 0, DM * DM * 2, 0x00020000);
    const int vw = (128 * wid + (lane >> 3)) * 2048 + nb * 128 + (((lane & 7) ^ (((lane >> 4) & 1) << 2)) << 4);
#pragma unroll
    for (int i = 0; i < 16; ++i) glds(rw, L + wid * 16384 + i * 1024, vw, i * 8 * 2048);
  }
  const char* os = ws + WS_O + (size_t)((4 * m + tl) * 32 + 16 * kh) * 2048 + lane * 16;
  bf16x8 a[32];
#pragma unroll
  for (int i = 0; i < 32; ++i) a[i] = *(const bf16x8*)(os + i * 1024);
  WAIT_V(32); BAR();
  const int q4 = (lane & 15) >> 2, pp = lane & 3, blk = (lane >> 4) & 1;
  const int sw = ((q4 >> 1) & 1) << 2;
  LAS const char* pw0 = L + (512 * kh + 4 * hi + q4) * 128 + (((2 * blk + (pp >> 1)) ^ sw) << 4) + 8 * (pp & 1);
  LAS const char* pw1 = L + (512 * kh + 4 * hi + q4) * 128 + (((4 + 2 * blk + (pp >> 1)) ^ sw) << 4) + 8 * (pp & 1);
  f32x16 c0 = f32x16{}, c1 = f32x16{};
#pragma unroll
  for (int i = 0; i < 32; ++i) {
    const bf16x8 b0 = cat(ldstr(pw0 + i * 2048), ldstr(pw0 + i * 2048 + 1024));
    const bf16x8 b1 = cat(ldstr(pw1 + i * 2048), ldstr(pw1 + i * 2048 + 1024));
    c0 = MFMA(b0, a[i], c0); c1 = MFMA(b1, a[i], c1);
  }
  LAS char* xs = L + 131072;
  {
    const f32x16 snd = kh ? c0 : c1;
    LAS float* d = (LAS float*)(xs + wid * 4096) + lane * 4;
#pragma unroll
    for (int g = 0; g < 4; ++g) *(LAS f32x4*)(d + g * 256) = (f32x4){snd[4 * g], snd[4 * g + 1], snd[4 * g + 2], snd[4 * g + 3]};
  }
  WAIT_L0(); BAR();
  f32x16 c = kh ? c1 : c0;
  {
    LAS const float* d = (LAS const float*)(xs + (wid ^ 4) * 4096) + lane * 4;
#pragma unroll
    for (int g = 0; g < 4; ++g) {
      const f32x4 v = *(LAS const f32x4*)(d + g * 256);
      c[4 * g] += v[0]; c[4 * g + 1] += v[1]; c[4 * g + 2] += v[2]; c[4 * g + 3] += v[3];
    }
  }
  char* od = out + (size_t)(128 * m + 32 * tl + (lane & 31)) * 2048 + nb * 128 + kh * 64 + hi * 16;
#pragma unroll
  for (int g2 = 0; g2 < 2; ++g2) {
    const unsigned a0 = pk2(c[8 * g2], c[8 * g2 + 1]), a1 = pk2(c[8 * g2 + 2], c[8 * g2 + 3]);
    const unsigned b0 = pk2(c[8 * g2 + 4], c[8 * g2 + 5]), b1 = pk2(c[8 * g2 + 6], c[8 * g2 + 7]);
    auto r0 = __builtin_amdgcn_permlane32_swap(a0, b0, false, false);
    auto r1 = __builtin_amdgcn_permlane32_swap(a1, b1, false, false);
    *(u32x4*)(od + g2 * 32) = (u32x4){r0[0], r1[0], r0[1], r1[1]};
  }
}

extern "C" void kernel_launch(void* const* d_in, const int* in_sizes, int n_in, void* d_out, int out_size, void* d_ws,
                              size_t ws_size, hipStream_t stream) {
  const char* x = (const char*)d_in[0];
  const char* wqkv = (const char*)d_in[1];
  const char* wo = (const char*)d_in[2];
  char* ws = (char*)d_ws;
  (void)hipFuncSetAttribute((const void*)k_qkv, hipFuncAttributeMaxDynamicSharedMemorySize, NSTG * STG);
  (void)hipFuncSetAttribute((const void*)k_attn, hipFuncAttributeMaxDynamicSharedMemorySize, 98304);
  (void)hipFuncSetAttribute((const void*)k_out, hipFuncAttributeMaxDynamicSharedMemorySize, 163840);
  k_qkv<<<256, 512, NSTG * STG, stream>>>(x, wqkv, ws);
  k_attn<<<256, 512, 98304, stream>>>(ws);
  k_out<<<256, 512, 163840, stream>>>(ws, wo, (char*)d_out);
}
```

```cpp
#include <hip/hip_runtime.h>
#include <hip/hip_bf16.h>

typedef short bf16x8 __attribute__((ext_vector_type(8)));
typedef short s16x4 __attribute__((ext_vector_type(4)));
typedef float f32x2 __attribute__((ext_vector_type(2)));
typedef float f32x4 __attribute__((ext_vector_type(4)));
typedef float f32x16 __attribute__((ext_vector_type(16)));
typedef unsigned u32x4 __attribute__((ext_vector_type(4)));
typedef __bf16 bf2_t __attribute__((ext_vector_type(2)));
#define LAS __attribute__((address_space(3)))
#define GAS __attribute__((address_space(1)))
typedef GAS unsigned long long gu64;
#define DI __device__ __forceinline__
#define M32(a, b, c) __builtin_amdgcn_mfma_f32_32x32x16_bf16((a), (b), (c), 0, 0, 0)
#define M16(a, b, c) __builtin_amdgcn_mfma_f32_16x16x32_bf16((a), (b), (c), 0, 0, 0)
#define SBAR() __builtin_amdgcn_sched_barrier(0)
#define BAR() asm volatile("s_barrier" ::: "memory")
#define WAIT_V(n) asm volatile("s_waitcnt vmcnt(%0)" ::"n"(n) : "memory")
#define WAIT_L0() asm volatile("s_waitcnt lgkmcnt(0)" ::: "memory")
#define LGKM0() __builtin_amdgcn_s_waitcnt(0xc07f)
#define RLX_AGENT __ATOMIC_RELAXED, __HIP_MEMORY_SCOPE_AGENT

#define STAMP(i)
#define KD 5
#define JF 7

constexpr int SEQ = 2048, DM = 1024;
constexpr int SLOT1 = 32768, SLOT2 = 24576, XSZ = 16384;
constexpr int L_REM = 24576, L_Q = 90112, L_OWN = 131072, L_XCH = 131072, L_ALL = 163840;
constexpr size_t WS_E = 0, WS_KVF = 4096, WS_OF = 8192, WS_KV = 1u << 20, WS_O = 16u << 20;
constexpr float QSCALE = 0.17677669529663687f * 1.4426950408889634f;
constexpr unsigned SPIN_CAP = 1u << 18;

DI unsigned pk2(float a, float b) { f32x2 f = {a, b}; return __builtin_bit_cast(unsigned, __builtin_convertvector(f, bf2_t)); }
DI u32x4 pack44(const f32x4& a, const f32x4& b) { return (u32x4){pk2(a[0], a[1]), pk2(a[2], a[3]), pk2(b[0], b[1]), pk2(b[2], b[3])}; }
DI u32x4 pack8(const f32x16& x, int s) {
  return (u32x4){pk2(x[8 * s], x[8 * s + 1]), pk2(x[8 * s + 2], x[8 * s + 3]), pk2(x[8 * s + 4], x[8 * s + 5]), pk2(x[8 * s + 6], x[8 * s + 7])};
}
DI bf16x8 asbf(u32x4 v) { return __builtin_bit_cast(bf16x8, v); }
DI bf16x8 lds128(LAS const char* p) { return *(LAS const bf16x8*)p; }
DI s16x4 ldstr(LAS const char* p) { return __builtin_amdgcn_ds_read_tr16_b64_v4i16((LAS s16x4*)p); }
DI bf16x8 cat(s16x4 a, s16x4 b) { return __builtin_shufflevector(a, b, 0, 1, 2, 3, 4, 5, 6, 7); }
DI void glds(__amdgpu_buffer_rsrc_t r, LAS char* dst, int voff, int soff) {
  __builtin_amdgcn_raw_ptr_buffer_load_lds(r, (LAS void*)dst, 16, voff, soff, 0, 0);
}
DI void glds_asm(__amdgpu_buffer_rsrc_t r, unsigned dst, int voff, int soff) {
  unsigned keep;
  asm volatile("s_mov_b32 %0, m0\n\ts_mov_b32 m0, %1\n\ts_nop 0\n\tbuffer_load_dwordx4 %2, %3, %4 offen lds\n\ts_mov_b32 m0, %0"
               : "=&s"(keep) : "s"(dst), "v"(voff), "s"(r), "s"(soff) : "memory");
}
#define MX3(a, b, c) __builtin_fmaxf(__builtin_fmaxf((a), (b)), (c))
DI float max16(const f32x16& p, float m) {
  float a = MX3(p[0], p[1], m), b = MX3(p[2], p[3], p[4]);
  a = MX3(a, p[5], p[6]); b = MX3(b, p[7], p[8]); a = MX3(a, p[9], p[10]); b = MX3(b, p[11], p[12]);
  a = MX3(a, p[13], p[14]); return MX3(a, b, p[15]);
}
constexpr unsigned long long diag_mask(int r) {
  const int k0 = (r & 3) + 8 * (r >> 3) + 16 * ((r >> 2) & 1);
  return (unsigned long long)(0xffffffffu << k0) | ((unsigned long long)(0xffffffffu << (k0 + 4)) << 32);
}
#define DIAG(r) __builtin_amdgcn_inverse_ballot_w64(diag_mask(r))

__global__ __launch_bounds__(512, 2) void k_fused(const char* __restrict__ x, const char* __restrict__ w, const char* __restrict__ wo,
                                                  char* __restrict__ ws, char* __restrict__ out) {
  extern __shared__ __attribute__((aligned(16))) char lds_[];
  LAS char* L = (LAS char*)lds_;
  const int bid = blockIdx.x, xcd = bid & 7, idx = bid >> 3;
  const int m = (xcd & 1) * 8 + (idx & 7), hp = (xcd >> 1) * 4 + (idx >> 3);
  const int lane = threadIdx.x & 63, wid = __builtin_amdgcn_readfirstlane(threadIdx.x >> 6);
  const int g = lane >> 4, q4 = (lane & 15) >> 2, pp = lane & 3;
  const __amdgpu_buffer_rsrc_t rkv = __builtin_amdgcn_make_buffer_rsrc((void*)(ws + WS_KV), 0, 8u << 20, 0x00020000);
  unsigned long long tag = 0;
  STAMP(0);
  if (wid >= 4) {
    const int lw = wid - 4;
    const __amdgpu_buffer_rsrc_t rx = __builtin_amdgcn_make_buffer_rsrc((void*)x, 0, SEQ * DM * 2, 0x00020000);
    const __amdgpu_buffer_rsrc_t rw = __builtin_amdgcn_make_buffer_rsrc((void*)w, 0, DM * 3 * DM * 2, 0x00020000);
    const int vx0 = (m * 128 + 32 * lw + (lane >> 3)) * 2048 + (((lane & 7) ^ (lane >> 4)) << 4), vx1 = vx0 ^ 64;
    const int vw0 = (16 * lw + (lane >> 3)) * 6144 + hp * 128 + (((lane & 7) ^ (((lane >> 4) & 1) << 1)) << 4), vw1 = vw0 ^ 64;
    auto issue1 = [&](int kt, int sb) {
      LAS char* d = L + sb;
#pragma unroll
      for (int i = 0; i < 4; ++i) glds(rx, d + lw * 4096 + i * 1024, (i & 1) ? vx1 : vx0, kt * 128 + i * 16384);
#pragma unroll
      for (int p = 0; p < 2; ++p)
#pragma unroll
        for (int i = 0; i < 2; ++i) glds(rw, d + XSZ + p * 8192 + lw * 2048 + i * 1024, i ? vw1 : vw0, kt * 64 * 6144 + (p + 1) * 2048 + i * 8 * 6144);
    };
    auto issue2 = [&](int kt, int sb) {
      LAS char* d = L + sb;
#pragma unroll
      for (int i = 0; i < 4; ++i) glds(rx, d + lw * 4096 + i * 1024, (i & 1) ? vx1 : vx0, kt * 128 + i * 16384);
#pragma unroll
      for (int i = 0; i < 2; ++i) glds(rw, d + XSZ + lw * 2048 + i * 1024, i ? vw1 : vw0, kt * 64 * 6144 + i * 8 * 6144);
    };
    issue1(0, 4 * SLOT1); issue1(1, 0);
    WAIT_V(8); BAR();
    STAMP(1);
    issue1(2, SLOT1); issue1(3, 2 * SLOT1);
    WAIT_V(16); BAR();
    issue1(4, 3 * SLOT1); issue1(5, 4 * SLOT1);
    WAIT_V(24); BAR();
    {
      int sb = 0;
#pragma unroll 1
      for (int j = 2; j < 12; ++j) {
        issue1(j + 4, sb);
        sb += SLOT1; if (sb == 5 * SLOT1) sb = 0;
        WAIT_V(24); BAR();
      }
    }
    STAMP(2);
    issue2(0, 0); WAIT_V(22); BAR();
    issue2(1, SLOT2); WAIT_V(20); BAR();
    issue2(2, 2 * SLOT2); WAIT_V(18); BAR();
    {
      int sb = 3 * SLOT2;
#pragma unroll 1
      for (int j = 3; j < 15; ++j) {
        issue2(j, sb);
        sb += SLOT2; if (sb == 5 * SLOT2) sb = 0;
        WAIT_V(18); BAR();
      }
    }
    issue2(15, 0);
    SBAR();
    STAMP(3);
    const int hl = lw & 1, pr = lw >> 1;
    const int src = ((2 * hp + hl) * 64 + 4 * max(m - 2 + pr, 0)) * 4096;
    u32x4 kv[16];
#pragma unroll
    for (int i = 0; i < 16; ++i) {
      const int o = (i & 1) * 1024 + ((i >> 1) & 3) * 4096 + (i >> 3) * 2048;
      kv[i] = __builtin_amdgcn_raw_buffer_load_b128(rkv, lane * 16, src + o, 16);
    }
    WAIT_V(34); BAR(); WAIT_V(28); BAR(); WAIT_V(22); BAR(); WAIT_V(16); BAR();
    STAMP(4);
    LAS char* d = L + L_REM + hl * 32768 + pr * 16384 + lane * 16;
#pragma unroll
    for (int i = 0; i < 16; ++i) {
      const int o = (i & 1) * 1024 + ((i >> 1) & 3) * 4096 + (i >> 3) * 2048;
      *(LAS u32x4*)(d + o) = kv[i];
    }
    STAMP(5);
  } else {
    const int wm = wid & 1, wn = wid >> 1;
    gu64* kvf = (gu64*)(ws + WS_KVF) + hp * 16;
    if (wid == 0) {
      const unsigned long long e = __hip_atomic_load((gu64*)(ws + WS_E), RLX_AGENT);
      const unsigned lo = __builtin_amdgcn_readfirstlane((unsigned)e), hi = __builtin_amdgcn_readfirstlane((unsigned)(e >> 32));
      tag = (((unsigned long long)hi << 32) | lo) * 0x9E3779B97F4A7C15ull + 0x632BE59BD9B4E019ull;
      asm volatile("" : "+s"(tag));
    }
    const int ax0 = (64 * wm + (lane & 15)) * 128 + ((g ^ ((lane >> 1) & 7)) << 4), ax1 = ax0 ^ 64;
    const int fw = ((q4 >> 1) & 1) | ((g & 1) << 1);
    const int aw0 = XSZ + (8 * g + q4) * 128 + (((2 * wn) ^ fw) << 5) + 8 * pp;
    const int aw1 = XSZ + (8 * g + q4) * 128 + (((2 * wn + 1) ^ fw) << 5) + 8 * pp;
    f32x4 acc[4][4];
#pragma unroll
    for (int i = 0; i < 4; ++i)
#pragma unroll
      for (int j = 0; j < 4; ++j) { acc[i][j] = f32x4{}; asm volatile("" : "+v"(acc[i][j])); }
    bf16x8 xf[2][4];
    s16x4 wl[2][4], wh[2][4];
#define WF(b, j) cat(wl[b][j], wh[b][j])
#define RX(n, i) xf[n][i] = lds128(px + (i) * 2048)
#define RL(n, j, s) wl[n][j] = ldstr(((j) & 1 ? pw1 : pw0) + ((j) >> 1) * 8192 + (s) * 4096)
#define RH(n, j, s) wh[n][j] = ldstr(((j) & 1 ? pw1 : pw0) + ((j) >> 1) * 8192 + (s) * 4096 + 512)
#define MM(b, i, j) acc[i][j] = ((j) < 2) ? M16(WF(b, j), xf[b][i], acc[i][j]) : M16(xf[b][i], WF(b, j), acc[i][j])
#define G(mm, rd) mm; SBAR(); rd; SBAR();
#define STEP1(b, n, s)                                                                                        \
  G(MM(b, 0, 0), RX(n, 0)) G(MM(b, 1, 0), RL(n, 0, s)) G(MM(b, 2, 0), RH(n, 0, s)) G(MM(b, 3, 0), RX(n, 1))   \
  G(MM(b, 0, 1), RX(n, 2)) G(MM(b, 1, 1), RX(n, 3)) G(MM(b, 2, 1), RL(n, 1, s)) G(MM(b, 3, 1), RH(n, 1, s))   \
  G(MM(b, 0, 2), RL(n, 2, s)) G(MM(b, 1, 2), RH(n, 2, s)) G(MM(b, 2, 2), RL(n, 3, s)) G(MM(b, 3, 2), RH(n, 3, s))
#define TAIL1(b) MM(b, 0, 3); MM(b, 1, 3); MM(b, 2, 3); MM(b, 3, 3); SBAR(); LGKM0(); SBAR();
    asm volatile("" ::"s"(ws));
    int sb = 4 * SLOT1;
    LAS const char* px = L + sb + ax0;
    LAS const char* pw0 = L + sb + aw0;
    LAS const char* pw1 = L + sb + aw1;
    BAR();
    STAMP(1);
    RX(0, 0); RL(0, 0, 0); RH(0, 0, 0); RX(0, 1); RX(0, 2); RX(0, 3); RL(0, 1, 0); RH(0, 1, 0); RL(0, 2, 0); RH(0, 2, 0); RL(0, 3, 0); RH(0, 3, 0);
    px = L + sb + ax1;
    LGKM0(); SBAR();
#pragma unroll 1
    for (int t = 0; t < 15; ++t) {
      STEP1(0, 1, 1)
      sb += SLOT1; if (sb == 5 * SLOT1) sb = 0;
      px = L + sb + ax0; pw0 = L + sb + aw0; pw1 = L + sb + aw1;
      TAIL1(0)
      BAR(); SBAR();
      STEP1(1, 0, 0)
      px = L + sb + ax1;
      TAIL1(1)
    }
    STEP1(0, 1, 1) TAIL1(0)
    BAR(); SBAR();
    STAMP(2);
    bf16x8 xq[2][8];
    s16x4 ql[2][4], qh[2][4];
    LAS const char *px0 = L + ax0, *px1 = L + ax1;
    pw0 = L + aw0; pw1 = L + aw1;
#define RXQ(n, i) xq[n][i] = lds128(((i) & 4 ? px1 : px0) + ((i) & 3) * 2048)
#define RQL(n, j) ql[n][j] = ldstr(((j) & 1 ? pw1 : pw0) + ((j) >> 1) * 4096)
#define RQH(n, j) qh[n][j] = ldstr(((j) & 1 ? pw1 : pw0) + ((j) >> 1) * 4096 + 512)
    G(MM(1, 0, 0), RXQ(0, 0)) G(MM(1, 1, 0), RQL(0, 0)) G(MM(1, 2, 0), RQH(0, 0)) G(MM(1, 3, 0), RXQ(0, 1))
    G(MM(1, 0, 1), RXQ(0, 2)) G(MM(1, 1, 1), RXQ(0, 3)) G(MM(1, 2, 1), RQL(0, 1)) G(MM(1, 3, 1), RQH(0, 1))
    G(MM(1, 0, 2), RXQ(0, 4)) G(MM(1, 1, 2), RQL(0, 2)) G(MM(1, 2, 2), RQH(0, 2)) G(MM(1, 3, 2), RXQ(0, 5))
    G(MM(1, 0, 3), RXQ(0, 6)) G(MM(1, 1, 3), RXQ(0, 7)) G(MM(1, 2, 3), RQL(0, 3)) G(MM(1, 3, 3), RQH(0, 3))
    {
      const int h = 2 * hp + wn;
      const int rho = 16 * ((lane >> 3) & 1) + (lane & 7);
      const int ok = (g >> 1) * 1024 + (rho + 32 * (g & 1)) * 16, ov = 2048 + (g >> 1) * 1024 + ((lane & 15) + 32 * (g & 1)) * 16;
      LAS char* dl = L + L_OWN + wn * 16384 + wm * 8192;
      const int dg = (h * 64 + 4 * m + 2 * wm) * 4096;
#pragma unroll
      for (int i = 0; i < 4; ++i) {
        const u32x4 v = pack44(acc[i][0], acc[i][1]);
        const int o = (i >> 1) * 4096 + (i & 1) * 128;
        __builtin_amdgcn_raw_buffer_store_b128(v, rkv, ok, dg + o, 16);
        *(LAS u32x4*)(dl + ok + o) = v;
      }
#pragma unroll
      for (int i = 0; i < 4; ++i) {
        const u32x4 v = pack44(acc[(i >> 1) * 2][2 + (i & 1)], acc[(i >> 1) * 2 + 1][2 + (i & 1)]);
        const int o = (i >> 1) * 4096 + (i & 1) * 256;
        __builtin_amdgcn_raw_buffer_store_b128(v, rkv, ov, dg + o, 16);
        *(LAS u32x4*)(dl + ov + o) = v;
      }
    }
    f32x4 aq[4][2];
#pragma unroll
    for (int i = 0; i < 4; ++i) { aq[i][0] = f32x4{}; aq[i][1] = f32x4{}; }
    LGKM0(); SBAR();
    STAMP(3);
#define QF(b, j) cat(ql[b][j], qh[b][j])
#define MQ(b, i, j) aq[(i) & 3][(j) & 1] = M16(QF(b, j), xq[b][((j) & 2) * 2 + ((i) & 3)], aq[(i) & 3][(j) & 1])
#define G2(mm, r0, r1) mm; SBAR(); r0; r1; SBAR();
#define STEP2(b, n)                                                                                           \
  G2(MQ(b, 0, 0), RXQ(n, 0), RQL(n, 0)) G2(MQ(b, 1, 0), RQH(n, 0), RXQ(n, 1)) G2(MQ(b, 2, 0), RXQ(n, 2), RXQ(n, 3)) \
  G2(MQ(b, 3, 0), RQL(n, 1), RQH(n, 1)) G2(MQ(b, 0, 1), RXQ(n, 4), RQL(n, 2)) G2(MQ(b, 1, 1), RQH(n, 2), RXQ(n, 5)) \
  G2(MQ(b, 2, 1), RXQ(n, 6), RXQ(n, 7)) G2(MQ(b, 3, 1), RQL(n, 3), RQH(n, 3))                                 \
  MQ(b, 0, 2); MQ(b, 1, 2); MQ(b, 2, 2); MQ(b, 3, 2); MQ(b, 0, 3); MQ(b, 1, 3); MQ(b, 2, 3); MQ(b, 3, 3); SBAR(); LGKM0(); SBAR();
    unsigned long long seen = 0;
    gu64* fsrc = kvf + max(m - 2 + (lane & 1), 0);
    auto phase_head = [&](int j) {
      sb += SLOT2; if (sb == 5 * SLOT2) sb = 0;
      px0 = L + sb + ax0; px1 = L + sb + ax1; pw0 = L + sb + aw0; pw1 = L + sb + aw1;
      if (j == KD) WAIT_V(0);
      if (wid == 0) {
        if (j == JF) seen = __hip_atomic_load(fsrc, RLX_AGENT);
        if (j == 10 && m > 0) {
          for (unsigned spins = 0; !__all(seen == tag) && spins < SPIN_CAP; ++spins) {
            __builtin_amdgcn_s_sleep(1);
            seen = __hip_atomic_load(fsrc, RLX_AGENT);
          }
          STAMP(8);
        }
      }
      SBAR(); BAR(); SBAR();
      if (j == KD && wid == 0 && lane == 0) __hip_atomic_store(kvf + m, tag, RLX_AGENT);
    };
    sb = 0;
#pragma unroll 1
    for (int j = 0; j < 14; j += 2) {
      phase_head(j); STEP2(0, 1)
      phase_head(j + 1); STEP2(1, 0)
    }
    phase_head(14); STEP2(0, 1)
#pragma unroll
    for (int j = 0; j < 4; ++j)
#pragma unroll
      for (int i = 0; i < 4; ++i) MQ(1, i, j);
    STAMP(4);
    LAS char* dq = L + L_Q + wn * 8192 + wm * 4096 + (g >> 1) * 1024 + ((lane & 15) + 32 * (g & 1)) * 16;
#pragma unroll
    for (int i = 0; i < 4; ++i) *(LAS u32x4*)(dq + (i >> 1) * 2048 + (i & 1) * 256) = pack44(aq[i][0] * QSCALE, aq[i][1] * QSCALE);
    STAMP(5);
  }
  __builtin_amdgcn_s_waitcnt(0x0070); BAR();
  STAMP(6);
  const int m2 = 2 * xcd + (idx & 1), nb = idx >> 1;
  const __amdgpu_buffer_rsrc_t rwo = __builtin_amdgcn_make_buffer_rsrc((void*)wo, 0, DM * DM * 2, 0x00020000);
  const int vwo = (lane >> 3) * 2048 + nb * 128 + (((lane & 7) ^ (((lane >> 4) & 3) << 1)) << 4);
  const unsigned lbase = (unsigned)(size_t)L;
  {
#pragma unroll
    for (int i = 0; i < 6; ++i) {
      const int pc = wid * 6 + i, p2 = pc < 24 ? pc : pc + 80;
      glds_asm(rwo, lbase + p2 * 1024, vwo, p2 * 16384);
    }
  }
  const int hl = wid >> 2, tl = wid & 3, hi = lane >> 5;
  const __amdgpu_buffer_rsrc_t ro = __builtin_amdgcn_make_buffer_rsrc((void*)(ws + WS_O), 0, 4u << 20, 0x00020000);
  {
    LAS const char* qs = L + L_Q + hl * 8192 + tl * 2048 + lane * 16;
    const bf16x8 q0 = lds128(qs), q1 = lds128(qs + 1024);
    const int rem = L_REM + hl * 32768 + tl * 4096, own = L_OWN + hl * 16384 + (tl - 8) * 4096;
    LAS const char* Ll = L + lane * 16;
    const f32x16 zero = f32x16{};
    const short one = (((lane >> 4) ^ lane) & 1) ? (short)0 : (short)0x3F80;
    const bf16x8 ones = {one, one, one, one, one, one, one, one};
    f32x16 o = zero;
    f32x4 ls = f32x4{};
    if (m >= 2) {
      LAS const char* kp[9];
#pragma unroll
      for (int j = 0; j < 9; ++j) kp[j] = Ll + ((j < 5 || (j < 8 && tl + j < 8)) ? rem : own) + 4096 * j;
      bf16x8 kf[9][2];
#pragma unroll
      for (int j = 8; j >= 0; --j) { kf[j][0] = lds128(kp[j]); kf[j][1] = lds128(kp[j] + 1024); }
      float mx = -INFINITY;
      f32x16 sd = M32(kf[8][0], q0, zero); sd = M32(kf[8][1], q1, sd);
#pragma unroll
      for (int j = 7; j >= 1; --j) {
        f32x16 s = M32(kf[j][0], q0, zero); s = M32(kf[j][1], q1, s);
        mx = max16(s, mx);
      }
      {
        f32x16 sf = M32(kf[0][0], q0, zero); sf = M32(kf[0][1], q1, sf);
#pragma unroll
        for (int r = 0; r < 16; ++r) sf[r] = DIAG(r) ? sd[r] : sf[r];
        mx = max16(sf, mx);
      }
      { auto rr = __builtin_amdgcn_permlane32_swap(__float_as_uint(mx), __float_as_uint(mx), false, false);
        mx = __builtin_fmaxf(__uint_as_float(rr[0]), __uint_as_float(rr[1])); }
      STAMP(7);
      f32x16 nm;
#pragma unroll
      for (int r = 0; r < 16; ++r) nm[r] = -mx;
#pragma unroll
      for (int j = 7; j >= 1; --j) {
        const bf16x8 v0 = lds128(kp[j] + 2048), v1 = lds128(kp[j] + 3072);
        f32x16 s = M32(kf[j][0], q0, nm); s = M32(kf[j][1], q1, s);
#pragma unroll
        for (int r = 0; r < 16; ++r) s[r] = __builtin_amdgcn_exp2f(s[r]);
        const bf16x8 p0 = asbf(pack8(s, 0)), p1 = asbf(pack8(s, 1));
        o = M32(v0, p0, o); o = M32(v1, p1, o);
        ls = M16(ones, p0, ls); ls = M16(ones, p1, ls);
      }
      {
        const bf16x8 vd0 = lds128(kp[8] + 2048), vd1 = lds128(kp[8] + 3072), vf0 = lds128(kp[0] + 2048), vf1 = lds128(kp[0] + 3072);
        f32x16 s = M32(kf[8][0], q0, nm); s = M32(kf[8][1], q1, s);
        f32x16 sf = M32(kf[0][0], q0, nm); sf = M32(kf[0][1], q1, sf);
#pragma unroll
        for (int r = 0; r < 16; ++r) s[r] = __builtin_amdgcn_exp2f(DIAG(r) ? s[r] : sf[r]);
        const u32x4 p0 = pack8(s, 0), p1 = pack8(s, 1);
        u32x4 d0, d1;
#pragma unroll
        for (int i = 0; i < 4; ++i) {
          d0[i] = p0[i] & (DIAG(2 * i + 1) ? 0xffffffffu : (DIAG(2 * i) ? 0xffffu : 0u));
          d1[i] = p1[i] & (DIAG(8 + 2 * i + 1) ? 0xffffffffu : (DIAG(8 + 2 * i) ? 0xffffu : 0u));
        }
        ls = M16(ones, asbf(p0), ls); ls = M16(ones, asbf(p1), ls);
        o = M32(vd0, asbf(d0), o); o = M32(vd1, asbf(d1), o);
        o = M32(vf0, asbf(p0 ^ d0), o); o = M32(vf1, asbf(p1 ^ d1), o);
      }
    } else {
      const int j0 = 8 - (4 * m + tl);
      float mx = -INFINITY;
      for (int j = j0; j <= 8; ++j) {
        LAS const char* kp = Ll + (tl + j < 8 ? rem : own) + 4096 * j;
        f32x16 s = M32(lds128(kp), q0, zero); s = M32(lds128(kp + 1024), q1, s);
        if (j == 8) {
#pragma unroll
          for (int r = 0; r < 16; ++r) s[r] = DIAG(r) ? s[r] : -INFINITY;
        }
        mx = max16(s, mx);
      }
      { auto rr = __builtin_amdgcn_permlane32_swap(__float_as_uint(mx), __float_as_uint(mx), false, false);
        mx = __builtin_fmaxf(__uint_as_float(rr[0]), __uint_as_float(rr[1])); }
      for (int j = j0; j <= 8; ++j) {
        LAS const char* kp = Ll + (tl + j < 8 ? rem : own) + 4096 * j;
        f32x16 s = M32(lds128(kp), q0, zero); s = M32(lds128(kp + 1024), q1, s);
        if (j == 8) {
#pragma unroll
          for (int r = 0; r < 16; ++r) s[r] = DIAG(r) ? s[r] : -INFINITY;
        }
#pragma unroll
        for (int r = 0; r < 16; ++r) s[r] = __builtin_amdgcn_exp2f(s[r] - mx);
        const bf16x8 p0 = asbf(pack8(s, 0)), p1 = asbf(pack8(s, 1));
        o = M32(lds128(kp + 2048), p0, o); o = M32(lds128(kp + 3072), p1, o);
        ls = M16(ones, p0, ls); ls = M16(ones, p1, ls);
      }
    }
    const float rl = __builtin_amdgcn_rcpf(((lane >> 4) & 1) ? ls[1] : ls[0]);
#pragma unroll
    for (int r = 0; r < 16; ++r) o[r] *= rl;
    const int vo = ((lane >> 4) & 1) * 1024 + hi * 256 + (lane & 15) * 16, so = ((4 * m + tl) * 32 + 2 * hp + hl) * 2048;
    __builtin_amdgcn_raw_buffer_store_b128(pack8(o, 0), ro, vo, so, 16);
    __builtin_amdgcn_raw_buffer_store_b128(pack8(o, 1), ro, vo + 512, so, 16);
  }
  STAMP(9);
  WAIT_V(0); WAIT_L0(); BAR();
  STAMP(10);
  if (wid == 0 && lane == 0) __hip_atomic_store((gu64*)(ws + WS_OF) + m * 16 + hp, tag, RLX_AGENT);
#pragma unroll
  for (int i = 0; i < 10; ++i) { const int p2 = 24 + wid * 10 + i; glds_asm(rwo, lbase + p2 * 1024, vwo, p2 * 16384); }
  if (wid == 0) {
    gu64* f = (gu64*)(ws + WS_OF) + m2 * 16 + (lane & 15);
    for (unsigned spins = 0; !__all(__hip_atomic_load(f, RLX_AGENT) == tag) && spins < SPIN_CAP; ++spins) __builtin_amdgcn_s_sleep(1);
  }
  STAMP(11);
  WAIT_V(0); BAR();
  STAMP(12);
  {
    bf16x8 a[32];
    const int so = ((4 * m2 + tl) * 32 + 16 * hl) * 2048;
#pragma unroll
    for (int i = 0; i < 32; ++i) a[i] = asbf(__builtin_amdgcn_raw_buffer_load_b128(ro, lane * 16, so + i * 1024, 16));
    SBAR();
    const int sw = 2 * (g & 1) + (q4 >> 1);
    LAS const char* pw = L + (512 * hl + 16 * (g >> 1) + 4 * (g & 1) + q4) * 128 + 8 * pp;
    f32x4 c[2][4];
#pragma unroll
    for (int u = 0; u < 2; ++u)
#pragma unroll
      for (int j = 0; j < 4; ++j) c[u][j] = f32x4{};
#pragma unroll
    for (int i = 0; i < 16; ++i) {
#pragma unroll
      for (int j = 0; j < 4; ++j) {
        LAS const char* p = pw + ((j ^ sw) << 5) + i * 4096;
        const bf16x8 b = cat(ldstr(p), ldstr(p + 1024));
        c[0][j] = M16(b, a[2 * i], c[0][j]); c[1][j] = M16(b, a[2 * i + 1], c[1][j]);
      }
    }
    STAMP(13);
    LAS char* xs = L + L_XCH + lane * 16;
#pragma unroll
    for (int j = 0; j < 4; ++j) *(LAS f32x4*)(xs + wid * 4096 + j * 1024) = hl ? c[0][j] : c[1][j];
    WAIT_L0(); BAR();
    f32x4 r[4];
#pragma unroll
    for (int j = 0; j < 4; ++j) r[j] = (hl ? c[1][j] : c[0][j]) + *(LAS const f32x4*)(xs + (wid ^ 4) * 4096 + j * 1024);
    char* od = out + (size_t)(128 * m2 + 32 * tl + 16 * hl + (lane & 15)) * 2048 + nb * 128 + (g & 1) * 32 + (g >> 1) * 16;
#pragma unroll
    for (int j = 0; j < 4; j += 2) {
      auto r0 = __builtin_amdgcn_permlane16_swap(pk2(r[j][0], r[j][1]), pk2(r[j + 1][0], r[j + 1][1]), false, false);
      auto r1 = __builtin_amdgcn_permlane16_swap(pk2(r[j][2], r[j][3]), pk2(r[j + 1][2], r[j + 1][3]), false, false);
      *(u32x4*)(od + j * 32) = (u32x4){r0[0], r1[0], r0[1], r1[1]};
    }
  }
  STAMP(14);
  if (bid == 0 && wid == 0) {
    gu64* f = (gu64*)(ws + WS_KVF) + lane;
    for (unsigned spins = 0; spins < SPIN_CAP; ++spins) {
      const bool ok = __hip_atomic_load(f, RLX_AGENT) == tag && __hip_atomic_load(f + 64, RLX_AGENT) == tag &&
                      __hip_atomic_load(f + 128, RLX_AGENT) == tag && __hip_atomic_load(f + 192, RLX_AGENT) == tag;
      if (__all(ok)) break;
      __builtin_amdgcn_s_sleep(1);
    }
    if (lane == 0) __hip_atomic_store((gu64*)(ws + WS_E), tag, RLX_AGENT);
  }
  STAMP(15);
}

extern "C" void kernel_launch(void* const* d_in, const int* in_sizes, int n_in, void* d_out, int out_size, void* d_ws,
                              size_t ws_size, hipStream_t stream) {
  (void)hipFuncSetAttribute((const void*)k_fused, hipFuncAttributeMaxDynamicSharedMemorySize, L_ALL);
  k_fused<<<256, 512, L_ALL, stream>>>((const char*)d_in[0], (const char*)d_in[1], (const char*)d_in[2], (char*)d_ws, (char*)d_out);
}
```

```cpp
#include <hip/hip_runtime.h>
#include <hip/hip_bf16.h>
#include <utility>

typedef short bf16x8 __attribute__((ext_vector_type(8)));
typedef short s16x2 __attribute__((ext_vector_type(2)));
typedef short s16x4 __attribute__((ext_vector_type(4)));
typedef float f32x2 __attribute__((ext_vector_type(2)));
typedef float f32x4 __attribute__((ext_vector_type(4)));
typedef float f32x16 __attribute__((ext_vector_type(16)));
typedef unsigned u32x4 __attribute__((ext_vector_type(4)));
typedef __bf16 bf2_t __attribute__((ext_vector_type(2)));
typedef __amdgpu_buffer_rsrc_t rsrc_t;
#define LAS __attribute__((address_space(3)))
#define GAS __attribute__((address_space(1)))
typedef GAS unsigned long long gu64;
#define DI __device__ __forceinline__
#define M32(a, b, c) __builtin_amdgcn_mfma_f32_32x32x16_bf16((a), (b), (c), 0, 0, 0)
#define M16(a, b, c) __builtin_amdgcn_mfma_f32_16x16x32_bf16((a), (b), (c), 0, 0, 0)
#define SBAR() __builtin_amdgcn_sched_barrier(0)
#define BAR() asm volatile("s_barrier" ::: "memory")
#define WAIT_V(n) asm volatile("s_waitcnt vmcnt(%0)" ::"n"(n) : "memory")
#define WAIT_VL(n) asm volatile("s_waitcnt vmcnt(%0) lgkmcnt(0)" ::"n"(n) : "memory")
#define WAIT_L0() asm volatile("s_waitcnt lgkmcnt(0)" ::: "memory")
#define LGKM0() __builtin_amdgcn_s_waitcnt(0xc07f)
#define VM0() __builtin_amdgcn_s_waitcnt(0x0f70)
#define PRIO(n) __builtin_amdgcn_s_setprio(n)
#define RLX_AGENT __ATOMIC_RELAXED, __HIP_MEMORY_SCOPE_AGENT

#define STAMP(i)
#define TSV(slot)
#define FS(i)
#define FSOUT(n)
#define ROT 2
#define X3P 2
#define FSSET 0
#define CPF 1
#define NA1 2

constexpr int SEQ = 2048, DM = 1024, KB = 1024;
constexpr int xslot(int t) { return 32 * KB + 16 * KB * (t % 6); }
constexpr int wslot(int t) { return t % 3 == 0 ? 0 : t % 3 == 1 ? 144 * KB : 16 * KB; }
constexpr int pslot(int s) { return s == 0 || s == 4 ? 128 * KB : s == 1 || s == 7 ? 144 * KB : s == 2 ? 16 * KB : s == 3 ? 0 : s == 5 ? 96 * KB : 112 * KB; }
constexpr int L_V = 0, L_K = 48 * KB, L_Q = 128 * KB, L_WE = 96 * KB, L_WO = 48 * KB, L_XCH = 96 * KB, L_ALL = 160 * KB;
constexpr int WROW = 6144, WTILE = 64 * WROW;
constexpr size_t WS_E = 0, WS_KVF = 4096, WS_OF = 16384, WS_KV = 1u << 20, WS_O = 16u << 20;
constexpr float QSCALE = 0.17677669529663687f * 1.4426950408889634f;
constexpr unsigned SPIN_CAP = 1u << 18;

template <class F, int... I> DI void sfor_(F&& f, std::integer_sequence<int, I...>) { (f(std::integral_constant<int, I>{}), ...); }
template <int N, class F> DI void sfor(F&& f) { sfor_(f, std::make_integer_sequence<int, N>{}); }

DI unsigned pk2(float a, float b) { f32x2 f = {a, b}; return __builtin_bit_cast(unsigned, __builtin_convertvector(f, bf2_t)); }
DI u32x4 pack44(const f32x4& a, const f32x4& b) { return (u32x4){pk2(a[0], a[1]), pk2(a[2], a[3]), pk2(b[0], b[1]), pk2(b[2], b[3])}; }
DI u32x4 pack8(const f32x16& x, int s) {
  return (u32x4){pk2(x[8 * s], x[8 * s + 1]), pk2(x[8 * s + 2], x[8 * s + 3]), pk2(x[8 * s + 4], x[8 * s + 5]), pk2(x[8 * s + 6], x[8 * s + 7])};
}
DI bf16x8 asbf(u32x4 v) { return __builtin_bit_cast(bf16x8, v); }
DI bf16x8 lds128(LAS const char* p) { return *(LAS const bf16x8*)p; }
DI s16x4 ldstr(LAS const char* p) { return __builtin_amdgcn_ds_read_tr16_b64_v4i16((LAS s16x4*)p); }
DI bf16x8 cat(s16x4 a, s16x4 b) { return __builtin_shufflevector(a, b, 0, 1, 2, 3, 4, 5, 6, 7); }
#define DMA4_(AUX)                                                                                             \
  asm volatile("s_add_u32 m0, %0, %1\n\ts_nop 0\n\t"                                                           \
               "buffer_load_dwordx4 %2, %4, %5 offen" AUX " lds\n\t"                                           \
               "buffer_load_dwordx4 %3, %4, %5 offen offset:1024" AUX " lds\n\t"                               \
               "buffer_load_dwordx4 %2, %4, %6 offen offset:2048" AUX " lds\n\t"                               \
               "buffer_load_dwordx4 %3, %4, %6 offen offset:3072" AUX " lds"                                   \
               ::"s"(base), "n"(OFF), "v"(v0), "v"(v1), "s"(r), "s"(s0), "s"(s1) : "memory", "scc")
template <int OFF> DI void dma4(rsrc_t r, unsigned base, int v0, int v1, int s0, int s1) { DMA4_(""); }
template <int OFF> DI void dma4c(rsrc_t r, unsigned base, int v0, int v1, int s0, int s1) { DMA4_(" sc1"); }
DI unsigned long long read_tag(const char* ws) {
  unsigned long long e;
  asm volatile("s_load_dwordx2 %0, %1, 0x0\n\ts_waitcnt lgkmcnt(0)" : "=s"(e) : "s"(ws) : "memory");
  e = e * 0x9E3779B97F4A7C15ull + 0x632BE59BD9B4E019ull;
  asm volatile("" : "+s"(e));
  return e;
}
#define MX3(a, b, c) __builtin_fmaxf(__builtin_fmaxf((a), (b)), (c))
constexpr unsigned long long diag_mask(int r) {
  const int k0 = (r & 3) + 8 * (r >> 3) + 16 * ((r >> 2) & 1);
  return (unsigned long long)(0xffffffffu << k0) | ((unsigned long long)(0xffffffffu << (k0 + 4)) << 32);
}
#define IC(n) std::integral_constant<int, n>{}
#define OPQ(p) asm volatile("" : "+v"(p))
#define MOVE(p, d) do { p += (d); OPQ(p); } while (0)
#define LANE() int lane = threadIdx.x & 63; OPQ(lane); const int g = lane >> 4, q4 = (lane & 15) >> 2, pp = lane & 3; (void)g; (void)q4; (void)pp
#define FA(i) FS(i)
#define FG(i)
#define FP(i)

__global__ __launch_bounds__(512, 2) void k_fused(const char* __restrict__ x, const char* __restrict__ w, const char* __restrict__ wo,
                                                  char* __restrict__ ws, char* __restrict__ out) {
  extern __shared__ __attribute__((aligned(16))) char lds_[];
  LAS char* L = (LAS char*)lds_;
  const unsigned lbase = (unsigned)(size_t)L;
  unsigned long long pc0;
  asm volatile("s_getpc_b64 %0\n\tkstart_lbl:" : "=s"(pc0));
  asm volatile("" ::"s"(x), "s"(w), "s"(wo), "s"(ws), "s"(out));
  const int bid = blockIdx.x, xcd = bid & 7, idx = bid >> 3;
  const int m = (xcd & 1) * 8 + (idx & 7), hp = (xcd >> 1) * 4 + (idx >> 3);
  const int wid = __builtin_amdgcn_readfirstlane(threadIdx.x >> 6), w4 = wid & 3;
  const int kc = idx + (idx >> 3), kg = idx & 4;
  auto ktile = [&](int t) {
    if (!ROT) return t;
    const int a1 = t < 2 || t >= 14 ? 0 : (t + 2) & 12, b1 = t < 2 || t >= 14 ? t : t + 2;
    return ROT == 1 ? (t + 4 * kc) & 15 : (kg ? a1 : t & 12) + (((kg ? b1 : t) + kc) & 3);
  };
  const rsrc_t rw = __builtin_amdgcn_make_buffer_rsrc((void*)(w - 2048), 0, DM * 3 * DM * 2 + 2048, 0x00020000);
  const unsigned lb4 = lbase + w4 * 4096;
  unsigned long long tag;
  int vQ0, vQ1;
  auto issueP = [&](auto sc) {
    constexpr int s = sc;
    dma4<pslot(s)>(rw, lb4, vQ0, vQ1, ktile((10 + 2 * s) & 15) * WTILE + 2048, ktile((11 + 2 * s) & 15) * WTILE);
  };
  const int hl = wid >> 2, tl = w4;
  LAS const char *qs, *vb, *kb;
  u32x4 mk[2];
  bf16x8 ones;
  unsigned long long dm[16];
  auto prep = [&]() {
    LANE();
    qs = L + L_Q + hl * 8192 + tl * 2048 + lane * 16;
    vb = L + L_V + hl * 24576 + tl * 2048 + lane * 16;
    kb = vb + L_K;
    const short dq = (short)((lane & 31) - 4 * (lane >> 5));
    const s16x2 d2 = {dq, dq};
#pragma unroll
    for (int i = 0; i < 8; ++i) {
      const short kk = (short)(((2 * i) & 3) + 8 * (i >> 2) + 16 * ((i >> 1) & 1));
      mk[i >> 2][i & 3] = __builtin_bit_cast(unsigned, (s16x2)(((s16x2){(short)(kk - 1), kk} - d2) >> 15));
    }
    const short one = (((lane >> 4) ^ lane) & 1) ? (short)0 : (short)0x3F80;
    ones = (bf16x8){one, one, one, one, one, one, one, one};
    asm volatile("" : "+v"(mk[0]), "+v"(mk[1]), "+v"(ones), "+v"(qs), "+v"(vb), "+v"(kb));
#pragma unroll
    for (int r = 0; r < 16; ++r) { dm[r] = diag_mask(r); asm volatile("" : "+s"(dm[r])); }
  };
  STAMP(0);
  if (__builtin_expect(wid >= 4, 1)) {
    PRIO(2);
    LANE();
    const rsrc_t rx = __builtin_amdgcn_make_buffer_rsrc((void*)x, 0, SEQ * DM * 2, 0x00020000);
    const int vX0 = (m * 128 + 32 * w4 + (lane >> 3)) * 2048 + (((lane & 7) ^ (lane >> 4)) << 4), vX1 = (vX0 ^ 64) + 16384 - 1024;
    auto issueX = [&](auto tc) { constexpr int t = tc; const int s0 = ktile(t) * 128; dma4<xslot(t)>(rx, lb4, vX0, vX1, s0, s0 + 32768 - 2048); };
    issueX(IC(0));
    SBAR();
    const int vA = (16 * w4 + (lane >> 3)) * WROW + hp * 128 + (((lane & 7) ^ (((lane >> 4) & 1) << 1)) << 4);
    const int vB = (vA ^ 64) + 8 * WROW - 1024;
    auto issueW = [&](auto tc) { constexpr int t = tc; const int s0 = ktile(t) * WTILE + 4096; dma4<wslot(t)>(rw, lb4, vA, vB, s0, s0); };
    issueW(IC(0)); issueX(IC(1)); issueW(IC(1));
    SBAR();
    tag = read_tag(ws);
    LAS const char* px[4];
#pragma unroll
    for (int s = 0; s < 4; ++s) { px[s] = L + 32 * KB + (32 * w4 + (lane & 31)) * 128 + (((2 * s + (lane >> 5)) ^ ((lane >> 1) & 7)) << 4); OPQ(px[s]); }
#define XO(t) (16 * KB * ((t) % 6 < 4 ? (t) % 6 : (t) % 6 - 4))
#define MOVEX(d) do { MOVE(px[0], d); MOVE(px[1], d); MOVE(px[2], d); MOVE(px[3], d); } while (0)
    bf16x8 xh[10][4];
    WAIT_V(8); BAR();
    STAMP(1);
    sfor<14>([&](auto tc) {
      constexpr int t = tc;
      issueX(IC(t + 2)); issueW(IC(t + 2));
      if constexpr (t == 13) issueP(IC(0));
      if constexpr (t == 4 || t == 10) MOVEX(64 * KB);
      if constexpr (t == 6) MOVEX(-64 * KB);
      if constexpr (t == 12) { vQ0 = vA ^ (((lane >> 4) & 1) * 0x60); vQ1 = vQ0 + 8 * WROW - 1024; }
      if constexpr (t < 10) {
#pragma unroll
        for (int s = 0; s < 4; ++s) xh[t][s] = lds128(px[s] + XO(t));
      }
      SBAR();
      WAIT_VL(t == 13 ? 12 : 8); TSV(t + 1); BAR();
    });
    issueP(IC(1)); WAIT_V(8); TSV(15); BAR();
    issueP(IC(2));
    bf16x8 xl[3];
    s16x4 fl[6], fh[6];
    f32x16 aq[2];
    aq[0] = f32x16{}; aq[1] = f32x16{}; OPQ(aq[0]); OPQ(aq[1]);
    xl[0] = lds128(px[0] + XO(10)); xl[1] = lds128(px[1] + XO(10));
    LAS const char* pq[2];
    pq[0] = L + 96 * KB + (8 * (g >> 1) + q4) * 128 + (((g & 1) ^ (q4 & 2)) << 5) + 8 * pp;
    pq[1] = L + 96 * KB + (8 * (g >> 1) + q4) * 128 + (((g & 1) ^ (q4 & 2) ^ 2) << 5) + 8 * pp;
    OPQ(pq[0]); OPQ(pq[1]);
#define PO(n) (pslot((n) >> 4) % (96 * KB) + (((n) >> 3) & 1) * 2048 + (((n) >> 1) & 3) * 4096)
    SBAR();
    WAIT_V(8); TSV(16); BAR();
    PRIO(0);
    STAMP(2);
    FP(0);
    auto rdw = [&](auto nc) { constexpr int n = nc; fl[n % 6] = ldstr(pq[n & 1] + PO(n)); fh[n % 6] = ldstr(pq[n & 1] + PO(n) + 512); };
    auto rdx = [&](auto sc) { constexpr int st = sc; xl[st % 3] = lds128(px[st & 3] + XO(10 + (st >> 2))); };
    sfor<5>([&](auto nc) { rdw(nc); SBAR(); });
    sfor<128>([&](auto nc) {
      constexpr int n = nc, u = n >> 3, st = n >> 1, h = n & 1;
      constexpr int nb = (n | 15) + 1, d = nb - n;
      constexpr bool cut = nb <= 80;
      constexpr int r0 = !cut || d > 5 ? n + 5 : d == 3 ? nb : d == 2 ? nb + 2 : d == 1 ? nb + 4 : 999;
      constexpr int r1 = cut && (d == 3 || d == 2) ? r0 + 1 : 999;
      aq[h] = M32(cat(fl[n % 6], fh[n % 6]), u < 6 ? xl[st % 3] : xh[u < 6 ? 0 : u - 6][st & 3], aq[h]); SBAR();
      if constexpr (r0 < 128) rdw(IC(r0));
      if constexpr (r1 < 128) rdw(IC(r1));
      if constexpr (n == 11) MOVEX(-64 * KB);
      if constexpr (n == 27 || n == 59) { MOVE(pq[0], n == 27 ? -96 * KB : 96 * KB); MOVE(pq[1], n == 27 ? -96 * KB : 96 * KB); }
      if constexpr (cut && d == 4) {
        SBAR();
        if constexpr (nb == 16) WAIT_V(4);
        if constexpr (nb == 32) WAIT_V(0);
        LGKM0(); SBAR(); FP(2 * (nb / 16) - 1); BAR(); FP(2 * (nb / 16)); SBAR();
      }
      if constexpr (h == 0 && st + 2 < 24) rdx(IC(st + 2));
      SBAR();
    });
    FP(11);
    STAMP(3);
    LAS char* dq = L + L_Q + w4 * 2048 + lane * 16;
#pragma unroll
    for (int h = 0; h < 2; ++h) {
      aq[h] *= QSCALE;
#pragma unroll
      for (int c = 0; c < 2; ++c)
        *(LAS u32x4*)(dq + h * 8192 + c * 1024) = (u32x4){pk2(aq[h][4 * c], aq[h][4 * c + 1]), pk2(aq[h][4 * c + 2], aq[h][4 * c + 3]),
                                                          pk2(aq[h][8 + 4 * c], aq[h][9 + 4 * c]), pk2(aq[h][10 + 4 * c], aq[h][11 + 4 * c])};
    }
    WAIT_L0();
    FP(12);
  } else {
    LANE();
    const int wm = wid & 1, wn = wid >> 1;
    unsigned pf = 0;
    if (wid == 0) {
      const unsigned o = (unsigned)(lane * 32 + idx) * 128u;
      unsigned long long keep;
      asm volatile("v_cmp_gt_u32 vcc, kend_lbl - kstart_lbl, %3\n\ts_and_saveexec_b64 %1, vcc\n\t"
                   "global_load_dword %0, %3, %2\n\ts_mov_b64 exec, %1"
                   : "+v"(pf), "=&s"(keep) : "s"(pc0), "v"(o) : "vcc", "scc", "memory");
    }
    tag = read_tag(ws);
    const int ax0 = (64 * wm + (lane & 15)) * 128 + ((g ^ ((lane >> 1) & 7)) << 4), ax1 = ax0 ^ 64;
    const int aw0 = (g >> 1) * 4096 + (8 * (g & 1) + q4) * 128 + (((2 * wn) ^ ((q4 >> 1) & 1) ^ ((g & 1) << 1)) << 5) + 8 * pp, aw1 = aw0 ^ 32;
    vQ0 = (16 * w4 + (lane >> 3)) * WROW + hp * 128 + (((lane & 7) ^ (((lane >> 4) & 1) << 2)) << 4); vQ1 = vQ0 + 8 * WROW - 1024;
    int ok = (g >> 1) * 1024 + (16 * ((lane >> 3) & 1) + (lane & 7) + 32 * (g & 1)) * 16, ov = (g >> 1) * 1024 + ((lane & 15) + 32 * (g & 1)) * 16;
    asm volatile("" : "+v"(vQ0), "+v"(vQ1), "+v"(ok), "+v"(ov));
    f32x4 acc[4][4];
#pragma unroll
    for (int i = 0; i < 4; ++i)
#pragma unroll
      for (int j = 0; j < 4; ++j) { acc[i][j] = f32x4{}; OPQ(acc[i][j]); }
    bf16x8 xf[2][4];
    s16x4 wl[2][4], wh[2][4];
#define WF(b, j) cat(wl[b][j], wh[b][j])
#define RX(n, i) xf[n][i] = lds128(px + (i) * 2048)
#define RL(n, j, s) wl[n][j] = ldstr(((j) & 1 ? pw1 : pw0) + ((j) >> 1) * 2048 + (s) * 8192)
#define RH(n, j, s) wh[n][j] = ldstr(((j) & 1 ? pw1 : pw0) + ((j) >> 1) * 2048 + (s) * 8192 + 512)
#define MM(b, i, j) acc[i][j] = ((j) < 2) ? M16(WF(b, j), xf[b][i], acc[i][j]) : M16(xf[b][i], WF(b, j), acc[i][j])
#define G(mm, rd) mm; SBAR(); rd; SBAR();
#define STEP1(b, n, s)                                                                                        \
  G(MM(b, 0, 0), RX(n, 0)) G(MM(b, 1, 0), RL(n, 0, s)) G(MM(b, 2, 0), RH(n, 0, s)) G(MM(b, 3, 0), RX(n, 1))   \
  G(MM(b, 0, 1), RX(n, 2)) G(MM(b, 1, 1), RX(n, 3)) G(MM(b, 2, 1), RL(n, 1, s)) G(MM(b, 3, 1), RH(n, 1, s))   \
  G(MM(b, 0, 2), RL(n, 2, s)) G(MM(b, 1, 2), RH(n, 2, s)) G(MM(b, 2, 2), RL(n, 3, s)) G(MM(b, 3, 2), RH(n, 3, s))
#define TAIL1(b) MM(b, 0, 3); MM(b, 1, 3); MM(b, 2, 3); MM(b, 3, 3); SBAR(); LGKM0(); SBAR();
    int xs = xslot(0), wsl = wslot(0);
    LAS const char* px = L + xs + ax0;
    LAS const char* pw0 = L + wsl + aw0;
    LAS const char* pw1 = L + wsl + aw1;
    BAR();
    STAMP(1);
    RX(0, 0); RL(0, 0, 0); RH(0, 0, 0); RX(0, 1); RX(0, 2); RX(0, 3); RL(0, 1, 0); RH(0, 1, 0); RL(0, 2, 0); RH(0, 2, 0); RL(0, 3, 0); RH(0, 3, 0);
    px = L + xs + ax1;
    LGKM0(); SBAR();
#pragma unroll 1
    for (int t = 0; t < 15; ++t) {
      STEP1(0, 1, 1)
      xs += 16 * KB; if (xs == 128 * KB) xs = 32 * KB;
      wsl = wsl == 0 ? 144 * KB : wsl == 16 * KB ? 0 : 16 * KB;
      px = L + xs + ax0; pw0 = L + wsl + aw0; pw1 = L + wsl + aw1;
      TAIL1(0)
      asm volatile("s_waitcnt vmcnt(0)" ::"v"(pf) : "memory");
      TSV(t + 1); BAR(); SBAR();
      STEP1(1, 0, 0)
      px = L + xs + ax1;
      TAIL1(1)
    }
    STEP1(0, 1, 1) TAIL1(0)
    TSV(16); BAR(); SBAR();
    PRIO(2);
    STAMP(2);
    FP(0);
    MM(1, 0, 0); MM(1, 0, 1); MM(1, 1, 0); MM(1, 1, 1); SBAR();
    issueP(IC(3)); SBAR();
    MM(1, 2, 0); MM(1, 2, 1); MM(1, 3, 0); MM(1, 3, 1);
    MM(1, 0, 2); MM(1, 1, 2); MM(1, 0, 3); MM(1, 1, 3); MM(1, 2, 2); MM(1, 3, 2); MM(1, 2, 3); MM(1, 3, 3);
    const rsrc_t rkv = __builtin_amdgcn_make_buffer_rsrc((void*)(ws + WS_KV), 0, 8u << 20, 0x00020000);
    const int dg = ((2 * hp + wn) * 64 + 4 * m + 2 * wm) * 4096;
    u32x4 kp[4], vp[4];
#pragma unroll
    for (int i = 0; i < 4; ++i) {
      kp[i] = pack44(acc[i][0], acc[i][1]);
      __builtin_amdgcn_raw_buffer_store_b128(kp[i], rkv, ok + (i & 1) * 128, dg + (i >> 1) * 4096, 16);
    }
#pragma unroll
    for (int i = 0; i < 4; ++i) {
      vp[i] = pack44(acc[(i >> 1) * 2][2 + (i & 1)], acc[(i >> 1) * 2 + 1][2 + (i & 1)]);
      __builtin_amdgcn_raw_buffer_store_b128(vp[i], rkv, ov + 2048 + (i & 1) * 256, dg + (i >> 1) * 4096, 16);
    }
    SBAR();
    FP(1); BAR(); FP(2);
    issueP(IC(4)); issueP(IC(5)); issueP(IC(6));
    FP(3); BAR(); FP(4);
    issueP(IC(7));
    prep();
    WAIT_V(24); FP(5); BAR(); FP(6);
    LAS char* dl = L + wn * 24576 + (8 + 2 * wm) * 2048;
#pragma unroll
    for (int i = 0; i < 4; ++i) *(LAS u32x4*)(dl + L_K + ok + (i >> 1) * 2048 + (i & 1) * 128) = kp[i];
    gu64* kvf = (gu64*)(ws + WS_KVF) + hp * 64;
    const int mb = max(m - 2 + wm, 0);
    gu64* fsrc = kvf + mb * 4 + wn * 2 + (lane & 1);
    WAIT_V(12);
    if (lane == 0) __hip_atomic_store(kvf + m * 4 + wid, tag, RLX_AGENT);
    FP(7); BAR(); FP(8);
    unsigned long long seen = __hip_atomic_load(fsrc, RLX_AGENT);
#pragma unroll
    for (int i = 0; i < 4; ++i) *(LAS u32x4*)(dl + L_V + ov + (i >> 1) * 2048 + (i & 1) * 256) = vp[i];
    WAIT_V(2); FP(9); BAR(); FP(10);
    for (unsigned spins = 0; !__all(seen == tag) && spins < SPIN_CAP; ++spins) seen = __hip_atomic_load(fsrc, RLX_AGENT);
    VM0();
    FP(11);
    STAMP(5);
    const int src = ((2 * hp + wn) * 64 + 4 * mb) * 4096, vl = lane * 16;
    const unsigned dr = lbase + wn * 24576 + wm * 8192;
    dma4c<L_K>(rkv, dr, vl, vl, src, src + 2048); dma4c<L_K + 4096>(rkv, dr, vl, vl, src + 8192, src + 10240);
    dma4c<L_V>(rkv, dr, vl, vl, src + 2048, src + 4096); dma4c<L_V + 4096>(rkv, dr, vl, vl, src + 10240, src + 12288);
    WAIT_VL(8);
    FP(12);
  }
  STAMP(6);
  BAR();
  STAMP(7);
  FA(0);
  if (hl) prep();
  const int m2 = 4 * (xcd & 3) + (idx & 3), nb = 8 * (xcd >> 2) + (idx >> 2), r4 = 4 * (idx & 3);
  const rsrc_t rwo = __builtin_amdgcn_make_buffer_rsrc((void*)wo, 0, DM * DM * 2, 0x00020000);
  const rsrc_t ro = __builtin_amdgcn_make_buffer_rsrc((void*)(ws + WS_O), 0, 4u << 20, 0x00020000);
#define ISSUE_WO(c, odd, dst) do { LANE(); const int v0_ = (lane >> 3) * 2048 + nb * 128 + (((lane & 7) ^ (((lane >> 4) & 3) << 1)) << 4); \
    const int s_ = (2 * ((4 * (c) + w4 + r4) & 15) + (odd)) * 65536; dma4<dst>(rwo, lb4, v0_, v0_ + 16384 - 1024, s_, s_ + 32768 - 2048); } while (0)
  {
    SBAR();
    const bf16x8 q0 = lds128(qs);
    bf16x8 kf[9][2];
#define RK(j) kf[j][0] = lds128(kb + (j) * 2048); kf[j][1] = lds128(kb + (j) * 2048 + 1024); SBAR();
    kf[8][0] = lds128(kb + 8 * 2048);
    const bf16x8 q1 = lds128(qs + 1024);
    kf[8][1] = lds128(kb + 8 * 2048 + 1024);
    SBAR();
    RK(0) RK(7)
    const int jmin = 8 - (4 * m + tl);
    const f32x16 zero = f32x16{};
    f32x16 s[2], sd;
    float mx, ma, mb_;
#define SA(d, j, c) d = M32(kf[j][0], q0, c); SBAR();
#define SB(d, j) d = M32(kf[j][1], q1, d); SBAR();
#define MXA(p) ma = MX3(p[0], p[1], p[2]); mb_ = MX3(p[3], p[4], p[5]); ma = MX3(ma, p[6], p[7]); mb_ = MX3(mb_, p[8], p[9]); SBAR();
#define MXB(p, j) ma = MX3(ma, p[10], p[11]); mb_ = MX3(mb_, p[12], p[13]); ma = MX3(ma, p[14], p[15]); \
    { unsigned pn_ = (j) >= jmin ? 0u : 0xff800000u; asm volatile("" : "+s"(pn_)); const float pen_ = __uint_as_float(pn_); \
      mx = MX3(mx, ma + pen_, mb_ + pen_); } SBAR();
#define MERGE(a, b) _Pragma("unroll") for (int r = (a); r < (b); ++r) sd[r] = __builtin_amdgcn_inverse_ballot_w64(dm[r]) ? sd[r] : s[0][r]; SBAR();
    FA(1);
    SA(sd, 8, zero) FA(2); RK(6) SB(sd, 8) RK(5) SA(s[0], 0, zero) RK(4) SB(s[0], 0) RK(3)
    SA(s[1], 7, zero) RK(2)
    if (__builtin_expect(jmin > 0, 0)) {
#pragma unroll
      for (int r = 0; r < 16; ++r) s[0][r] = -INFINITY;
      OPQ(s[0]);
    }
    SBAR();
    FA(3);
    SB(s[1], 7) RK(1) MERGE(0, 16)
    OPQ(sd);
    FA(4);
    if (hl) { ISSUE_WO(0, 0, L_WE); WAIT_L0(); BAR(); ISSUE_WO(2, 0, L_WE + 32 * KB); }
    SBAR();
    FA(5);
    SA(s[0], 6, zero) MXA(sd) SB(s[0], 6)
    ma = MX3(ma, sd[10], sd[11]); mb_ = MX3(mb_, sd[12], sd[13]); ma = MX3(ma, sd[14], sd[15]); mx = __builtin_fmaxf(ma, mb_); SBAR();
    MXA(s[1]) MXB(s[1], 7)
    FA(6);
    SA(s[1], 5, zero) MXA(s[0]) SB(s[1], 5) MXB(s[0], 6)
    SA(s[0], 4, zero) MXA(s[1]) SB(s[0], 4) MXB(s[1], 5)
    OPQ(mx);
    if (hl) ISSUE_WO(1, 0, L_WE + 16 * KB);
    SBAR();
    FA(7);
    SA(s[1], 3, zero) MXA(s[0]) SB(s[1], 3) MXB(s[0], 4)
    SA(s[0], 2, zero) MXA(s[1]) SB(s[0], 2) MXB(s[1], 3)
    OPQ(mx);
    if (hl) ISSUE_WO(3, 0, L_WE + 48 * KB);
    SBAR();
    FA(8);
    SA(s[1], 1, zero) MXA(s[0]) SB(s[1], 1) MXB(s[0], 2)
    FA(9);
    MXA(s[1]) MXB(s[1], 1)
    { auto rr = __builtin_amdgcn_permlane32_swap(__float_as_uint(mx), __float_as_uint(mx), false, false);
      mx = __builtin_fmaxf(__uint_as_float(rr[0]), __uint_as_float(rr[1])); }
    f32x16 nm;
#pragma unroll
    for (int r = 0; r < 16; ++r) nm[r] = -mx;
    OPQ(nm);
    FA(10);
    if (!hl) { WAIT_V(0); FA(11); BAR(); }
    SBAR();
    FA(12);
    bf16x8 vf[2][2], vx0, vx1;
    u32x4 p[2][2], pd[2];
    f32x16 o = zero;
    f32x4 ls = f32x4{};
    float e[16];
    unsigned pc[8];
#define EDGE(j) if (__builtin_expect((j) == jmin - 1, 0)) { _Pragma("unroll") for (int r = 0; r < 16; ++r) nm[r] = -INFINITY; OPQ(nm); } SBAR();
#define MEET(n) if ((n) == X3P) { if (hl) { WAIT_V(0); BAR(); } SBAR(); }
#define EX(b, r) e[r] = __builtin_amdgcn_exp2f(s[b][r]); OPQ(e[r]);
#define CV(b, i) pc[i] = pk2(e[2 * (i)], e[2 * (i) + 1]); if (((i) & 3) == 3) { p[b][(i) >> 2] = (u32x4){pc[(i) & 4], pc[((i) & 4) + 1], pc[((i) & 4) + 2], pc[((i) & 4) + 3]}; OPQ(p[b][(i) >> 2]); }
#define RV(d0, d1, j) d0 = lds128(vb + (j) * 2048); d1 = lds128(vb + (j) * 2048 + 1024);
    RV(vf[0][0], vf[0][1], 8) RV(vx0, vx1, 0)
    EDGE(7)
    SA(s[1], 7, nm)
#pragma unroll
    for (int r = 0; r < 16; ++r) s[0][r] = sd[r] - mx;
    OPQ(s[0]);
    SBAR();
    SB(s[1], 7)
#pragma unroll
    for (int r = 0; r < 16; ++r) { EX(0, r) }
#pragma unroll
    for (int i = 0; i < 8; ++i) { CV(0, i) }
    pd[0] = p[0][0] & mk[0]; pd[1] = p[0][1] & mk[1];
    OPQ(pd[0]); OPQ(pd[1]);
    SBAR();
    MEET(0)
    FA(13);
    EDGE(6)
    SA(s[0], 6, nm) EX(1, 0) EX(1, 1) EX(1, 2) SBAR();
    SB(s[0], 6) EX(1, 3) EX(1, 4) EX(1, 5) SBAR();
    ls = M16(ones, asbf(p[0][0]), ls); SBAR(); EX(1, 6) SBAR();
    ls = M16(ones, asbf(p[0][1]), ls); SBAR(); p[0][0] ^= pd[0]; SBAR();
    o = M32(vf[0][0], asbf(pd[0]), o); SBAR(); p[0][1] ^= pd[1]; EX(1, 7) SBAR();
    o = M32(vf[0][1], asbf(pd[1]), o); SBAR(); EX(1, 8) EX(1, 9) EX(1, 10) RV(vf[1][0], vf[1][1], 7) SBAR();
    o = M32(vx0, asbf(p[0][0]), o); SBAR(); EX(1, 11) EX(1, 12) EX(1, 13) SBAR();
    o = M32(vx1, asbf(p[0][1]), o); SBAR(); EX(1, 14) EX(1, 15)
    CV(1, 0) CV(1, 1) CV(1, 2) CV(1, 3) CV(1, 4) CV(1, 5) CV(1, 6) CV(1, 7) SBAR();
    MEET(1)
#define ITER(n, b, c)                                                                                         \
    EDGE(7 - (n))                                                                                             \
    SA(s[c], 7 - (n), nm) EX(b, 0) EX(b, 1) EX(b, 2) SBAR();                                                  \
    SB(s[c], 7 - (n)) EX(b, 3) EX(b, 4) EX(b, 5) SBAR();                                                      \
    ITAIL(n, b, c)
#define ITAIL(n, b, c)                                                                                        \
    o = M32(vf[c][0], asbf(p[c][0]), o); SBAR(); EX(b, 6) EX(b, 7) EX(b, 8) SBAR();                           \
    o = M32(vf[c][1], asbf(p[c][1]), o); SBAR(); EX(b, 9) EX(b, 10) EX(b, 11) RV(vf[b][0], vf[b][1], 8 - (n)) SBAR(); \
    ls = M16(ones, asbf(p[c][0]), ls); SBAR(); EX(b, 12) EX(b, 13) CV(b, 0) CV(b, 1) CV(b, 2) SBAR();         \
    ls = M16(ones, asbf(p[c][1]), ls); SBAR(); EX(b, 14) EX(b, 15) CV(b, 3) CV(b, 4) CV(b, 5) CV(b, 6) CV(b, 7) SBAR(); \
    MEET(n)
    FA(14); ITER(2, 0, 1) FA(15); ITER(3, 1, 0) FA(16); ITER(4, 0, 1) FA(17); ITER(5, 1, 0) FA(18); ITER(6, 0, 1) FA(19);
    EX(1, 0) EX(1, 1) EX(1, 2) EX(1, 3) EX(1, 4) EX(1, 5) SBAR();
    ITAIL(7, 1, 0)
    FA(20);
    ls = M16(ones, asbf(p[1][0]), ls); ls = M16(ones, asbf(p[1][1]), ls);
    o = M32(vf[1][0], asbf(p[1][0]), o); o = M32(vf[1][1], asbf(p[1][1]), o);
    SBAR();
    FA(21);
    LANE();
    const float rl = __builtin_amdgcn_rcpf((g & 1) ? ls[1] : ls[0]);
#pragma unroll
    for (int r = 0; r < 16; ++r) o[r] *= rl;
    const int vo = (g & 1) * 1024 + (g >> 1) * 256 + (lane & 15) * 16, so = ((4 * m + tl) * 32 + 2 * hp + hl) * 2048;
    __builtin_amdgcn_raw_buffer_store_b128(pack8(o, 0), ro, vo, so, 16);
    __builtin_amdgcn_raw_buffer_store_b128(pack8(o, 1), ro, vo + 512, so, 16);
  }
  FA(22);
  FG(0);
  PRIO(0);
  LANE();
  gu64* of = (gu64*)(ws + WS_OF);
  gu64* fe = of + (m2 * 4 + tl) * 32 + ((8 * hl + (lane & 7) + r4) & 15);
  unsigned long long seen;
  if (!hl) {
    BAR();
    FG(1);
    ISSUE_WO(0, 1, L_WO);
    if (NA1 > 1) ISSUE_WO(2, 1, L_WO + 32 * KB);
    if (NA1 > 2) ISSUE_WO(1, 1, L_WO + 16 * KB);
    if (NA1 > 3) ISSUE_WO(3, 1, 0);
    FG(2);
    WAIT_V(4 * NA1);
  } else {
    WAIT_V(0);
  }
  if (lane == 0) __hip_atomic_store(of + ((m * 4 + tl) * 2 + hl) * 16 + hp, tag, RLX_AGENT);
  FG(3);
  FSOUT(23);
  STAMP(11);
  if (!hl) {
    if (NA1 <= 1) ISSUE_WO(2, 1, L_WO + 32 * KB);
    if (NA1 <= 2) ISSUE_WO(1, 1, L_WO + 16 * KB);
    if (NA1 <= 3) ISSUE_WO(3, 1, 0);
  }
  {
    FG(4);
    seen = __hip_atomic_load(fe, RLX_AGENT);
    for (unsigned spins = 0; !__all(seen == tag) && spins < SPIN_CAP; ++spins) seen = __hip_atomic_load(fe, RLX_AGENT);
    FG(5);
    STAMP(12);
    bf16x8 a[16];
    const int vl = lane * 16, so = (4 * m2 + tl) * 65536;
    const int sa0 = so + ((8 * hl + r4) & 15) * 4096, sa1 = so + ((8 * hl + 4 + r4) & 15) * 4096;
    auto loadA = [&](int odd) {
#pragma unroll
      for (int i = 0; i < 16; ++i)
        a[i] = asbf(__builtin_amdgcn_raw_buffer_load_b128(ro, vl + (i & 1) * 1024 + odd * 2048, (i < 8 ? sa0 : sa1) + ((i >> 1) & 3) * 4096, 16));
    };
    loadA(0);
    seen = __hip_atomic_load(fe + 16, RLX_AGENT);
    SBAR();
    FG(6);
    const int sw = 2 * (g & 1) + (q4 >> 1);
    const int bw = (16 * (g >> 1) + 4 * (g & 1) + q4) * 128 + 8 * pp + (sw << 5);
    LAS const char* pb[4];
    f32x4 c[2][4];
    s16x4 bl[2][4], bh[2][4];
#pragma unroll
    for (int j = 0; j < 4; ++j) { c[0][j] = f32x4{}; c[1][j] = f32x4{}; pb[j] = L + L_WE + hl * 32768 + (bw ^ (j << 5)); OPQ(pb[j]); }
#define RB(n, j, off) bl[n][j] = ldstr(pb[j] + (off)); bh[n][j] = ldstr(pb[j] + (off) + 1024);
#define MB(b, j, i) c[0][j] = M16(cat(bl[b][j], bh[b][j]), a[2 * (i)], c[0][j]); c[1][j] = M16(cat(bl[b][j], bh[b][j]), a[2 * (i) + 1], c[1][j]); SBAR();
#define HEAD(b, n, i, off) MB(b, 0, i) RB(n, 0, off) SBAR(); MB(b, 1, i) RB(n, 1, off) SBAR(); MB(b, 2, i) RB(n, 2, off) SBAR(); MB(b, 3, i) RB(n, 3, off) SBAR();
#define HEADL(b, i) MB(b, 0, i) MB(b, 1, i) MB(b, 2, i) MB(b, 3, i)
    RB(0, 0, 0) RB(0, 1, 0) RB(0, 2, 0) RB(0, 3, 0) SBAR();
    HEAD(0, 1, 0, 4096) FG(7); HEAD(1, 0, 1, 8192) HEAD(0, 1, 2, 12288) HEAD(1, 0, 3, 16384) FG(8);
    HEAD(0, 1, 4, 20480) HEAD(1, 0, 5, 24576) HEAD(0, 1, 6, 28672) HEADL(1, 7)
    FG(9);
    for (unsigned spins = 0; !__all(seen == tag) && spins < SPIN_CAP; ++spins) seen = __hip_atomic_load(fe + 16, RLX_AGENT);
    FG(10);
    STAMP(14);
    loadA(1);
    SBAR();
    FG(11);
    BAR();
    FG(12);
#pragma unroll
    for (int j = 0; j < 4; ++j) MOVE(pb[j], L_WO - L_WE);
    RB(0, 0, 0) RB(0, 1, 0) RB(0, 2, 0) RB(0, 3, 0) SBAR();
    HEAD(0, 1, 0, 4096) FG(13); HEAD(1, 0, 1, 8192) HEAD(0, 1, 2, 12288)
    {
      const int dlt = hl ? -(L_WO + 32768) : 16384;
#pragma unroll
      for (int j = 0; j < 4; ++j) MOVE(pb[j], dlt);
      SBAR();
    }
    HEAD(1, 0, 3, 0) FG(14);
    HEAD(0, 1, 4, 4096) HEAD(1, 0, 5, 8192) HEAD(0, 1, 6, 12288) HEADL(1, 7)
    FG(15);
    LAS char* xs = L + L_XCH + lane * 16;
#pragma unroll
    for (int j = 0; j < 4; ++j) *(LAS f32x4*)(xs + wid * 4096 + j * 1024) = hl ? c[0][j] : c[1][j];
    WAIT_L0(); FG(16); BAR(); FG(17);
    f32x4 r[4];
#pragma unroll
    for (int j = 0; j < 4; ++j) r[j] = (hl ? c[1][j] : c[0][j]) + *(LAS const f32x4*)(xs + (wid ^ 4) * 4096 + j * 1024);
    char* od = out + (size_t)(128 * m2 + 32 * tl + 16 * hl + (lane & 15)) * 2048 + nb * 128 + (g & 1) * 32 + (g >> 1) * 16;
#pragma unroll
    for (int j = 0; j < 4; j += 2) {
      auto r0 = __builtin_amdgcn_permlane16_swap(pk2(r[j][0], r[j][1]), pk2(r[j + 1][0], r[j + 1][1]), false, false);
      auto r1 = __builtin_amdgcn_permlane16_swap(pk2(r[j][2], r[j][3]), pk2(r[j + 1][2], r[j + 1][3]), false, false);
      *(u32x4*)(od + j * 32) = (u32x4){r0[0], r1[0], r0[1], r1[1]};
    }
    FG(18);
  }
  STAMP(16);
  if (bid == 0 && wid == 0) {
    gu64* f = (gu64*)(ws + WS_KVF) + lane * 4;
    for (unsigned spins = 0; spins < SPIN_CAP; ++spins) {
      const bool ok = __hip_atomic_load(f, RLX_AGENT) == tag && __hip_atomic_load(f + 256, RLX_AGENT) == tag &&
                      __hip_atomic_load(f + 512, RLX_AGENT) == tag && __hip_atomic_load(f + 768, RLX_AGENT) == tag;
      if (__all(ok)) break;
      __builtin_amdgcn_s_sleep(1);
    }
    if (lane == 0) __hip_atomic_store((gu64*)(ws + WS_E), tag, RLX_AGENT);
  }
}

__global__ void k_end() { asm volatile("kend_lbl:"); }

extern "C" void kernel_launch(void* const* d_in, const int* in_sizes, int n_in, void* d_out, int out_size, void* d_ws,
                              size_t ws_size, hipStream_t stream) {
  (void)hipFuncSetAttribute((const void*)k_fused, hipFuncAttributeMaxDynamicSharedMemorySize, L_ALL);
  k_fused<<<256, 512, L_ALL, stream>>>((const char*)d_in[0], (const char*)d_in[1], (const char*)d_in[2], (char*)d_ws, (char*)d_out);
}
```

```cpp
#define MA 0
#define MZ 1
#define MK 4
#define MW 0
#define MBID 101
#include <hip/hip_runtime.h>
#include <hip/hip_bf16.h>
#include <utility>

typedef short bf16x8 __attribute__((ext_vector_type(8)));
typedef short s16x2 __attribute__((ext_vector_type(2)));
typedef short s16x4 __attribute__((ext_vector_type(4)));
typedef float f32x2 __attribute__((ext_vector_type(2)));
typedef float f32x4 __attribute__((ext_vector_type(4)));
typedef float f32x16 __attribute__((ext_vector_type(16)));
typedef unsigned u32x4 __attribute__((ext_vector_type(4)));
typedef __bf16 bf2_t __attribute__((ext_vector_type(2)));
typedef __amdgpu_buffer_rsrc_t rsrc_t;
#define LAS __attribute__((address_space(3)))
#define GAS __attribute__((address_space(1)))
typedef GAS unsigned long long gu64;
#define DI __device__ __forceinline__
#define M32(a, b, c) __builtin_amdgcn_mfma_f32_32x32x16_bf16((a), (b), (c), 0, 0, 0)
#define M16(a, b, c) __builtin_amdgcn_mfma_f32_16x16x32_bf16((a), (b), (c), 0, 0, 0)
#define SBAR() __builtin_amdgcn_sched_barrier(0)
#define BAR() asm volatile("s_barrier" ::: "memory")
#define WAIT_V(n) asm volatile("s_waitcnt vmcnt(%0)" ::"n"(n) : "memory")
#define WAIT_VL(n) asm volatile("s_waitcnt vmcnt(%0) lgkmcnt(0)" ::"n"(n) : "memory")
#define WAIT_L0() asm volatile("s_waitcnt lgkmcnt(0)" ::: "memory")
#define LGKM0() __builtin_amdgcn_s_waitcnt(0xc07f)
#define VM0() __builtin_amdgcn_s_waitcnt(0x0f70)
#define PRIO(n) __builtin_amdgcn_s_setprio(n)
#define RLX_AGENT __ATOMIC_RELAXED, __HIP_MEMORY_SCOPE_AGENT

#define MT() __builtin_amdgcn_s_memrealtime()
#define STAMP(i) do { if ((i) == MA) { mta = MT(); asm volatile("" : "+s"(mta)); } if ((i) == MZ) { mtb = MT(); asm volatile("" : "+s"(mtb)); } } while (0)
#define TSV(slot)
#define FS(i)
#define FSOUT(n)
#define ROT 2
#define X3P 2
#define FSSET 0
#define CPF 1
#define NA1 2

constexpr int SEQ = 2048, DM = 1024, KB = 1024;
constexpr int xslot(int t) { return 32 * KB + 16 * KB * (t % 6); }
constexpr int wslot(int t) { return t % 3 == 0 ? 0 : t % 3 == 1 ? 144 * KB : 16 * KB; }
constexpr int pslot(int s) { return s == 0 || s == 4 ? 128 * KB : s == 1 || s == 7 ? 144 * KB : s == 2 ? 16 * KB : s == 3 ? 0 : s == 5 ? 96 * KB : 112 * KB; }
constexpr int L_V = 0, L_K = 48 * KB, L_Q = 128 * KB, L_WE = 96 * KB, L_WO = 48 * KB, L_XCH = 96 * KB, L_ALL = 160 * KB;
constexpr int WROW = 6144, WTILE = 64 * WROW;
constexpr size_t WS_E = 0, WS_KVF = 4096, WS_OF = 16384, WS_KV = 1u << 20, WS_O = 16u << 20;
constexpr float QSCALE = 0.17677669529663687f * 1.4426950408889634f;
constexpr unsigned SPIN_CAP = 1u << 18;

template <class F, int... I> DI void sfor_(F&& f, std::integer_sequence<int, I...>) { (f(std::integral_constant<int, I>{}), ...); }
template <int N, class F> DI void sfor(F&& f) { sfor_(f, std::make_integer_sequence<int, N>{}); }

DI unsigned pk2(float a, float b) { f32x2 f = {a, b}; return __builtin_bit_cast(unsigned, __builtin_convertvector(f, bf2_t)); }
DI u32x4 pack44(const f32x4& a, const f32x4& b) { return (u32x4){pk2(a[0], a[1]), pk2(a[2], a[3]), pk2(b[0], b[1]), pk2(b[2], b[3])}; }
DI u32x4 pack8(const f32x16& x, int s) {
  return (u32x4){pk2(x[8 * s], x[8 * s + 1]), pk2(x[8 * s + 2], x[8 * s + 3]), pk2(x[8 * s + 4], x[8 * s + 5]), pk2(x[8 * s + 6], x[8 * s + 7])};
}
DI bf16x8 asbf(u32x4 v) { return __builtin_bit_cast(bf16x8, v); }
DI bf16x8 lds128(LAS const char* p) { return *(LAS const bf16x8*)p; }
DI s16x4 ldstr(LAS const char* p) { return __builtin_amdgcn_ds_read_tr16_b64_v4i16((LAS s16x4*)p); }
DI bf16x8 cat(s16x4 a, s16x4 b) { return __builtin_shufflevector(a, b, 0, 1, 2, 3, 4, 5, 6, 7); }
#define DMA4_(AUX)                                                                                             \
  asm volatile("s_add_u32 m0, %0, %1\n\ts_nop 0\n\t"                                                           \
               "buffer_load_dwordx4 %2, %4, %5 offen" AUX " lds\n\t"                                           \
               "buffer_load_dwordx4 %3, %4, %5 offen offset:1024" AUX " lds\n\t"                               \
               "buffer_load_dwordx4 %2, %4, %6 offen offset:2048" AUX " lds\n\t"                               \
               "buffer_load_dwordx4 %3, %4, %6 offen offset:3072" AUX " lds"                                   \
               ::"s"(base), "n"(OFF), "v"(v0), "v"(v1), "s"(r), "s"(s0), "s"(s1) : "memory", "scc")
template <int OFF> DI void dma4(rsrc_t r, unsigned base, int v0, int v1, int s0, int s1) { DMA4_(""); }
template <int OFF> DI void dma4c(rsrc_t r, unsigned base, int v0, int v1, int s0, int s1) { DMA4_(" sc1"); }
DI unsigned long long read_tag(const char* ws) {
  unsigned long long e;
  asm volatile("s_load_dwordx2 %0, %1, 0x0\n\ts_waitcnt lgkmcnt(0)" : "=s"(e) : "s"(ws) : "memory");
  e = e * 0x9E3779B97F4A7C15ull + 0x632BE59BD9B4E019ull;
  asm volatile("" : "+s"(e));
  return e;
}
#define MX3(a, b, c) __builtin_fmaxf(__builtin_fmaxf((a), (b)), (c))
constexpr unsigned long long diag_mask(int r) {
  const int k0 = (r & 3) + 8 * (r >> 3) + 16 * ((r >> 2) & 1);
  return (unsigned long long)(0xffffffffu << k0) | ((unsigned long long)(0xffffffffu << (k0 + 4)) << 32);
}
#define IC(n) std::integral_constant<int, n>{}
#define OPQ(p) asm volatile("" : "+v"(p))
#define MOVE(p, d) do { p += (d); OPQ(p); } while (0)
#define LANE() int lane = threadIdx.x & 63; OPQ(lane); const int g = lane >> 4, q4 = (lane & 15) >> 2, pp = lane & 3; (void)g; (void)q4; (void)pp
#define FA(i) FS(i)
#define FG(i)
#define FP(i)

__global__ __launch_bounds__(512, 2) void k_fused(const char* __restrict__ x, const char* __restrict__ w, const char* __restrict__ wo,
                                                  char* __restrict__ ws, char* __restrict__ out) {
  extern __shared__ __attribute__((aligned(16))) char lds_[];
  LAS char* L = (LAS char*)lds_;
  const unsigned lbase = (unsigned)(size_t)L;
  unsigned long long pc0;
  asm volatile("s_getpc_b64 %0\n\tkstart_lbl:" : "=s"(pc0));
  asm volatile("" ::"s"(x), "s"(w), "s"(wo), "s"(ws), "s"(out));
  const int bid = blockIdx.x, xcd = bid & 7, idx = bid >> 3;
  const int m = (xcd & 1) * 8 + (idx & 7), hp = (xcd >> 1) * 4 + (idx >> 3);
  const int wid = __builtin_amdgcn_readfirstlane(threadIdx.x >> 6), w4 = wid & 3;
  const int kc = idx + (idx >> 3), kg = idx & 4;
  auto ktile = [&](int t) {
    if (!ROT) return t;
    const int a1 = t < 2 || t >= 14 ? 0 : (t + 2) & 12, b1 = t < 2 || t >= 14 ? t : t + 2;
    return ROT == 1 ? (t + 4 * kc) & 15 : (kg ? a1 : t & 12) + (((kg ? b1 : t) + kc) & 3);
  };
  const rsrc_t rw = __builtin_amdgcn_make_buffer_rsrc((void*)(w - 2048), 0, DM * 3 * DM * 2 + 2048, 0x00020000);
  const unsigned lb4 = lbase + w4 * 4096;
  unsigned long long tag;
  unsigned long long mta = 0, mtb = 0;
  int vQ0, vQ1;
  auto issueP = [&](auto sc) {
    constexpr int s = sc;
    dma4<pslot(s)>(rw, lb4, vQ0, vQ1, ktile((10 + 2 * s) & 15) * WTILE + 2048, ktile((11 + 2 * s) & 15) * WTILE);
  };
  const int hl = wid >> 2, tl = w4;
  LAS const char *qs, *vb, *kb;
  u32x4 mk[2];
  bf16x8 ones;
  unsigned long long dm[16];
  auto prep = [&]() {
    LANE();
    qs = L + L_Q + hl * 8192 + tl * 2048 + lane * 16;
    vb = L + L_V + hl * 24576 + tl * 2048 + lane * 16;
    kb = vb + L_K;
    const short dq = (short)((lane & 31) - 4 * (lane >> 5));
    const s16x2 d2 = {dq, dq};
#pragma unroll
    for (int i = 0; i < 8; ++i) {
      const short kk = (short)(((2 * i) & 3) + 8 * (i >> 2) + 16 * ((i >> 1) & 1));
      mk[i >> 2][i & 3] = __builtin_bit_cast(unsigned, (s16x2)(((s16x2){(short)(kk - 1), kk} - d2) >> 15));
    }
    const short one = (((lane >> 4) ^ lane) & 1) ? (short)0 : (short)0x3F80;
    ones = (bf16x8){one, one, one, one, one, one, one, one};
    asm volatile("" : "+v"(mk[0]), "+v"(mk[1]), "+v"(ones), "+v"(qs), "+v"(vb), "+v"(kb));
#pragma unroll
    for (int r = 0; r < 16; ++r) { dm[r] = diag_mask(r); asm volatile("" : "+s"(dm[r])); }
  };
  STAMP(0);
  if (__builtin_expect(wid >= 4, 1)) {
    PRIO(2);
    LANE();
    const rsrc_t rx = __builtin_amdgcn_make_buffer_rsrc((void*)x, 0, SEQ * DM * 2, 0x00020000);
    const int vX0 = (m * 128 + 32 * w4 + (lane >> 3)) * 2048 + (((lane & 7) ^ (lane >> 4)) << 4), vX1 = (vX0 ^ 64) + 16384 - 1024;
    auto issueX = [&](auto tc) { constexpr int t = tc; const int s0 = ktile(t) * 128; dma4<xslot(t)>(rx, lb4, vX0, vX1, s0, s0 + 32768 - 2048); };
    issueX(IC(0));
    SBAR();
    const int vA = (16 * w4 + (lane >> 3)) * WROW + hp * 128 + (((lane & 7) ^ (((lane >> 4) & 1) << 1)) << 4);
    const int vB = (vA ^ 64) + 8 * WROW - 1024;
    auto issueW = [&](auto tc) { constexpr int t = tc; const int s0 = ktile(t) * WTILE + 4096; dma4<wslot(t)>(rw, lb4, vA, vB, s0, s0); };
    issueW(IC(0)); issueX(IC(1)); issueW(IC(1));
    SBAR();
    tag = read_tag(ws);
    LAS const char* px[4];
#pragma unroll
    for (int s = 0; s < 4; ++s) { px[s] = L + 32 * KB + (32 * w4 + (lane & 31)) * 128 + (((2 * s + (lane >> 5)) ^ ((lane >> 1) & 7)) << 4); OPQ(px[s]); }
#define XO(t) (16 * KB * ((t) % 6 < 4 ? (t) % 6 : (t) % 6 - 4))
#define MOVEX(d) do { MOVE(px[0], d); MOVE(px[1], d); MOVE(px[2], d); MOVE(px[3], d); } while (0)
    bf16x8 xh[10][4];
    WAIT_V(8); BAR();
    STAMP(1);
    sfor<14>([&](auto tc) {
      constexpr int t = tc;
      issueX(IC(t + 2)); issueW(IC(t + 2));
      if constexpr (t == 13) issueP(IC(0));
      if constexpr (t == 4 || t == 10) MOVEX(64 * KB);
      if constexpr (t == 6) MOVEX(-64 * KB);
      if constexpr (t == 12) { vQ0 = vA ^ (((lane >> 4) & 1) * 0x60); vQ1 = vQ0 + 8 * WROW - 1024; }
      if constexpr (t < 10) {
#pragma unroll
        for (int s = 0; s < 4; ++s) xh[t][s] = lds128(px[s] + XO(t));
      }
      SBAR();
      WAIT_VL(t == 13 ? 12 : 8); TSV(t + 1); BAR();
    });
    issueP(IC(1)); WAIT_V(8); TSV(15); BAR();
    issueP(IC(2));
    bf16x8 xl[3];
    s16x4 fl[6], fh[6];
    f32x16 aq[2];
    aq[0] = f32x16{}; aq[1] = f32x16{}; OPQ(aq[0]); OPQ(aq[1]);
    xl[0] = lds128(px[0] + XO(10)); xl[1] = lds128(px[1] + XO(10));
    LAS const char* pq[2];
    pq[0] = L + 96 * KB + (8 * (g >> 1) + q4) * 128 + (((g & 1) ^ (q4 & 2)) << 5) + 8 * pp;
    pq[1] = L + 96 * KB + (8 * (g >> 1) + q4) * 128 + (((g & 1) ^ (q4 & 2) ^ 2) << 5) + 8 * pp;
    OPQ(pq[0]); OPQ(pq[1]);
#define PO(n) (pslot((n) >> 4) % (96 * KB) + (((n) >> 3) & 1) * 2048 + (((n) >> 1) & 3) * 4096)
    SBAR();
    WAIT_V(8); TSV(16); BAR();
    PRIO(0);
    STAMP(2);
    FP(0);
    auto rdw = [&](auto nc) { constexpr int n = nc; fl[n % 6] = ldstr(pq[n & 1] + PO(n)); fh[n % 6] = ldstr(pq[n & 1] + PO(n) + 512); };
    auto rdx = [&](auto sc) { constexpr int st = sc; xl[st % 3] = lds128(px[st & 3] + XO(10 + (st >> 2))); };
    sfor<5>([&](auto nc) { rdw(nc); SBAR(); });
    sfor<128>([&](auto nc) {
      constexpr int n = nc, u = n >> 3, st = n >> 1, h = n & 1;
      constexpr int nb = (n | 15) + 1, d = nb - n;
      constexpr bool cut = nb <= 80;
      constexpr int r0 = !cut || d > 5 ? n + 5 : d == 3 ? nb : d == 2 ? nb + 2 : d == 1 ? nb + 4 : 999;
      constexpr int r1 = cut && (d == 3 || d == 2) ? r0 + 1 : 999;
      aq[h] = M32(cat(fl[n % 6], fh[n % 6]), u < 6 ? xl[st % 3] : xh[u < 6 ? 0 : u - 6][st & 3], aq[h]); SBAR();
      if constexpr (r0 < 128) rdw(IC(r0));
      if constexpr (r1 < 128) rdw(IC(r1));
      if constexpr (n == 11) MOVEX(-64 * KB);
      if constexpr (n == 27 || n == 59) { MOVE(pq[0], n == 27 ? -96 * KB : 96 * KB); MOVE(pq[1], n == 27 ? -96 * KB : 96 * KB); }
      if constexpr (cut && d == 4) {
        SBAR();
        if constexpr (nb == 16) WAIT_V(4);
        if constexpr (nb == 32) WAIT_V(0);
        LGKM0(); SBAR(); FP(2 * (nb / 16) - 1); BAR(); FP(2 * (nb / 16)); SBAR();
      }
      if constexpr (h == 0 && st + 2 < 24) rdx(IC(st + 2));
      SBAR();
    });
    FP(11);
    STAMP(3);
    LAS char* dq = L + L_Q + w4 * 2048 + lane * 16;
#pragma unroll
    for (int h = 0; h < 2; ++h) {
      aq[h] *= QSCALE;
#pragma unroll
      for (int c = 0; c < 2; ++c)
        *(LAS u32x4*)(dq + h * 8192 + c * 1024) = (u32x4){pk2(aq[h][4 * c], aq[h][4 * c + 1]), pk2(aq[h][4 * c + 2], aq[h][4 * c + 3]),
                                                          pk2(aq[h][8 + 4 * c], aq[h][9 + 4 * c]), pk2(aq[h][10 + 4 * c], aq[h][11 + 4 * c])};
    }
    WAIT_L0();
    FP(12);
  } else {
    LANE();
    const int wm = wid & 1, wn = wid >> 1;
    unsigned pf = 0;
    if (wid == 0) {
      const unsigned o = (unsigned)(lane * 32 + idx) * 128u;
      unsigned long long keep;
      asm volatile("v_cmp_gt_u32 vcc, kend_lbl - kstart_lbl, %3\n\ts_and_saveexec_b64 %1, vcc\n\t"
                   "global_load_dword %0, %3, %2\n\ts_mov_b64 exec, %1"
                   : "+v"(pf), "=&s"(keep) : "s"(pc0), "v"(o) : "vcc", "scc", "memory");
    }
    tag = read_tag(ws);
    const int ax0 = (64 * wm + (lane & 15)) * 128 + ((g ^ ((lane >> 1) & 7)) << 4), ax1 = ax0 ^ 64;
    const int aw0 = (g >> 1) * 4096 + (8 * (g & 1) + q4) * 128 + (((2 * wn) ^ ((q4 >> 1) & 1) ^ ((g & 1) << 1)) << 5) + 8 * pp, aw1 = aw0 ^ 32;
    vQ0 = (16 * w4 + (lane >> 3)) * WROW + hp * 128 + (((lane & 7) ^ (((lane >> 4) & 1) << 2)) << 4); vQ1 = vQ0 + 8 * WROW - 1024;
    int ok = (g >> 1) * 1024 + (16 * ((lane >> 3) & 1) + (lane & 7) + 32 * (g & 1)) * 16, ov = (g >> 1) * 1024 + ((lane & 15) + 32 * (g & 1)) * 16;
    asm volatile("" : "+v"(vQ0), "+v"(vQ1), "+v"(ok), "+v"(ov));
    f32x4 acc[4][4];
#pragma unroll
    for (int i = 0; i < 4; ++i)
#pragma unroll
      for (int j = 0; j < 4; ++j) { acc[i][j] = f32x4{}; OPQ(acc[i][j]); }
    bf16x8 xf[2][4];
    s16x4 wl[2][4], wh[2][4];
#define WF(b, j) cat(wl[b][j], wh[b][j])
#define RX(n, i) xf[n][i] = lds128(px + (i) * 2048)
#define RL(n, j, s) wl[n][j] = ldstr(((j) & 1 ? pw1 : pw0) + ((j) >> 1) * 2048 + (s) * 8192)
#define RH(n, j, s) wh[n][j] = ldstr(((j) & 1 ? pw1 : pw0) + ((j) >> 1) * 2048 + (s) * 8192 + 512)
#define MM(b, i, j) acc[i][j] = ((j) < 2) ? M16(WF(b, j), xf[b][i], acc[i][j]) : M16(xf[b][i], WF(b, j), acc[i][j])
#define G(mm, rd) mm; SBAR(); rd; SBAR();
#define STEP1(b, n, s)                                                                                        \
  G(MM(b, 0, 0), RX(n, 0)) G(MM(b, 1, 0), RL(n, 0, s)) G(MM(b, 2, 0), RH(n, 0, s)) G(MM(b, 3, 0), RX(n, 1))   \
  G(MM(b, 0, 1), RX(n, 2)) G(MM(b, 1, 1), RX(n, 3)) G(MM(b, 2, 1), RL(n, 1, s)) G(MM(b, 3, 1), RH(n, 1, s))   \
  G(MM(b, 0, 2), RL(n, 2, s)) G(MM(b, 1, 2), RH(n, 2, s)) G(MM(b, 2, 2), RL(n, 3, s)) G(MM(b, 3, 2), RH(n, 3, s))
#define TAIL1(b) MM(b, 0, 3); MM(b, 1, 3); MM(b, 2, 3); MM(b, 3, 3); SBAR(); LGKM0(); SBAR();
    int xs = xslot(0), wsl = wslot(0);
    LAS const char* px = L + xs + ax0;
    LAS const char* pw0 = L + wsl + aw0;
    LAS const char* pw1 = L + wsl + aw1;
    BAR();
    STAMP(1);
    RX(0, 0); RL(0, 0, 0); RH(0, 0, 0); RX(0, 1); RX(0, 2); RX(0, 3); RL(0, 1, 0); RH(0, 1, 0); RL(0, 2, 0); RH(0, 2, 0); RL(0, 3, 0); RH(0, 3, 0);
    px = L + xs + ax1;
    LGKM0(); SBAR();
#pragma unroll 1
    for (int t = 0; t < 15; ++t) {
      STEP1(0, 1, 1)
      xs += 16 * KB; if (xs == 128 * KB) xs = 32 * KB;
      wsl = wsl == 0 ? 144 * KB : wsl == 16 * KB ? 0 : 16 * KB;
      px = L + xs + ax0; pw0 = L + wsl + aw0; pw1 = L + wsl + aw1;
      TAIL1(0)
      asm volatile("s_waitcnt vmcnt(0)" ::"v"(pf) : "memory");
      TSV(t + 1); BAR(); SBAR();
      STEP1(1, 0, 0)
      px = L + xs + ax1;
      TAIL1(1)
    }
    STEP1(0, 1, 1) TAIL1(0)
    TSV(16); BAR(); SBAR();
    PRIO(2);
    STAMP(2);
    FP(0);
    MM(1, 0, 0); MM(1, 0, 1); MM(1, 1, 0); MM(1, 1, 1); SBAR();
    issueP(IC(3)); SBAR();
    MM(1, 2, 0); MM(1, 2, 1); MM(1, 3, 0); MM(1, 3, 1);
    MM(1, 0, 2); MM(1, 1, 2); MM(1, 0, 3); MM(1, 1, 3); MM(1, 2, 2); MM(1, 3, 2); MM(1, 2, 3); MM(1, 3, 3);
    const rsrc_t rkv = __builtin_amdgcn_make_buffer_rsrc((void*)(ws + WS_KV), 0, 8u << 20, 0x00020000);
    const int dg = ((2 * hp + wn) * 64 + 4 * m + 2 * wm) * 4096;
    u32x4 kp[4], vp[4];
#pragma unroll
    for (int i = 0; i < 4; ++i) {
      kp[i] = pack44(acc[i][0], acc[i][1]);
      __builtin_amdgcn_raw_buffer_store_b128(kp[i], rkv, ok + (i & 1) * 128, dg + (i >> 1) * 4096, 16);
    }
#pragma unroll
    for (int i = 0; i < 4; ++i) {
      vp[i] = pack44(acc[(i >> 1) * 2][2 + (i & 1)], acc[(i >> 1) * 2 + 1][2 + (i & 1)]);
      __builtin_amdgcn_raw_buffer_store_b128(vp[i], rkv, ov + 2048 + (i & 1) * 256, dg + (i >> 1) * 4096, 16);
    }
    SBAR();
    FP(1); BAR(); FP(2);
    issueP(IC(4)); issueP(IC(5)); issueP(IC(6));
    FP(3); BAR(); FP(4);
    issueP(IC(7));
    prep();
    WAIT_V(24); FP(5); BAR(); FP(6);
    LAS char* dl = L + wn * 24576 + (8 + 2 * wm) * 2048;
#pragma unroll
    for (int i = 0; i < 4; ++i) *(LAS u32x4*)(dl + L_K + ok + (i >> 1) * 2048 + (i & 1) * 128) = kp[i];
    gu64* kvf = (gu64*)(ws + WS_KVF) + hp * 64;
    const int mb = max(m - 2 + wm, 0);
    gu64* fsrc = kvf + mb * 4 + wn * 2 + (lane & 1);
    WAIT_V(12);
    if (lane == 0) __hip_atomic_store(kvf + m * 4 + wid, tag, RLX_AGENT);
    FP(7); BAR(); FP(8);
    unsigned long long seen = __hip_atomic_load(fsrc, RLX_AGENT);
#pragma unroll
    for (int i = 0; i < 4; ++i) *(LAS u32x4*)(dl + L_V + ov + (i >> 1) * 2048 + (i & 1) * 256) = vp[i];
    WAIT_V(2); FP(9); BAR(); FP(10);
    for (unsigned spins = 0; !__all(seen == tag) && spins < SPIN_CAP; ++spins) seen = __hip_atomic_load(fsrc, RLX_AGENT);
    VM0();
    FP(11);
    STAMP(5);
    const int src = ((2 * hp + wn) * 64 + 4 * mb) * 4096, vl = lane * 16;
    const unsigned dr = lbase + wn * 24576 + wm * 8192;
    dma4c<L_K>(rkv, dr, vl, vl, src, src + 2048); dma4c<L_K + 4096>(rkv, dr, vl, vl, src + 8192, src + 10240);
    dma4c<L_V>(rkv, dr, vl, vl, src + 2048, src + 4096); dma4c<L_V + 4096>(rkv, dr, vl, vl, src + 10240, src + 12288);
    WAIT_VL(8);
    FP(12);
  }
  STAMP(6);
  BAR();
  STAMP(7);
  FA(0);
  if (hl) prep();
  const int m2 = 4 * (xcd & 3) + (idx & 3), nb = 8 * (xcd >> 2) + (idx >> 2), r4 = 4 * (idx & 3);
  const rsrc_t rwo = __builtin_amdgcn_make_buffer_rsrc((void*)wo, 0, DM * DM * 2, 0x00020000);
  const rsrc_t ro = __builtin_amdgcn_make_buffer_rsrc((void*)(ws + WS_O), 0, 4u << 20, 0x00020000);
#define ISSUE_WO(c, odd, dst) do { LANE(); const int v0_ = (lane >> 3) * 2048 + nb * 128 + (((lane & 7) ^ (((lane >> 4) & 3) << 1)) << 4); \
    const int s_ = (2 * ((4 * (c) + w4 + r4) & 15) + (odd)) * 65536; dma4<dst>(rwo, lb4, v0_, v0_ + 16384 - 1024, s_, s_ + 32768 - 2048); } while (0)
  {
    SBAR();
    const bf16x8 q0 = lds128(qs);
    bf16x8 kf[9][2];
#define RK(j) kf[j][0] = lds128(kb + (j) * 2048); kf[j][1] = lds128(kb + (j) * 2048 + 1024); SBAR();
    kf[8][0] = lds128(kb + 8 * 2048);
    const bf16x8 q1 = lds128(qs + 1024);
    kf[8][1] = lds128(kb + 8 * 2048 + 1024);
    SBAR();
    RK(0) RK(7)
    const int jmin = 8 - (4 * m + tl);
    const f32x16 zero = f32x16{};
    f32x16 s[2], sd;
    float mx, ma, mb_;
#define SA(d, j, c) d = M32(kf[j][0], q0, c); SBAR();
#define SB(d, j) d = M32(kf[j][1], q1, d); SBAR();
#define MXA(p) ma = MX3(p[0], p[1], p[2]); mb_ = MX3(p[3], p[4], p[5]); ma = MX3(ma, p[6], p[7]); mb_ = MX3(mb_, p[8], p[9]); SBAR();
#define MXB(p, j) ma = MX3(ma, p[10], p[11]); mb_ = MX3(mb_, p[12], p[13]); ma = MX3(ma, p[14], p[15]); \
    { unsigned pn_ = (j) >= jmin ? 0u : 0xff800000u; asm volatile("" : "+s"(pn_)); const float pen_ = __uint_as_float(pn_); \
      mx = MX3(mx, ma + pen_, mb_ + pen_); } SBAR();
#define MERGE(a, b) _Pragma("unroll") for (int r = (a); r < (b); ++r) sd[r] = __builtin_amdgcn_inverse_ballot_w64(dm[r]) ? sd[r] : s[0][r]; SBAR();
    FA(1);
    SA(sd, 8, zero) FA(2); RK(6) SB(sd, 8) RK(5) SA(s[0], 0, zero) RK(4) SB(s[0], 0) RK(3)
    SA(s[1], 7, zero) RK(2)
    if (__builtin_expect(jmin > 0, 0)) {
#pragma unroll
      for (int r = 0; r < 16; ++r) s[0][r] = -INFINITY;
      OPQ(s[0]);
    }
    SBAR();
    FA(3);
    SB(s[1], 7) RK(1) MERGE(0, 16)
    OPQ(sd);
    FA(4);
    if (hl) { ISSUE_WO(0, 0, L_WE); WAIT_L0(); BAR(); ISSUE_WO(2, 0, L_WE + 32 * KB); }
    SBAR();
    FA(5);
    SA(s[0], 6, zero) MXA(sd) SB(s[0], 6)
    ma = MX3(ma, sd[10], sd[11]); mb_ = MX3(mb_, sd[12], sd[13]); ma = MX3(ma, sd[14], sd[15]); mx = __builtin_fmaxf(ma, mb_); SBAR();
    MXA(s[1]) MXB(s[1], 7)
    FA(6);
    SA(s[1], 5, zero) MXA(s[0]) SB(s[1], 5) MXB(s[0], 6)
    SA(s[0], 4, zero) MXA(s[1]) SB(s[0], 4) MXB(s[1], 5)
    OPQ(mx);
    if (hl) ISSUE_WO(1, 0, L_WE + 16 * KB);
    SBAR();
    FA(7);
    SA(s[1], 3, zero) MXA(s[0]) SB(s[1], 3) MXB(s[0], 4)
    SA(s[0], 2, zero) MXA(s[1]) SB(s[0], 2) MXB(s[1], 3)
    OPQ(mx);
    if (hl) ISSUE_WO(3, 0, L_WE + 48 * KB);
    SBAR();
    FA(8);
    SA(s[1], 1, zero) MXA(s[0]) SB(s[1], 1) MXB(s[0], 2)
    FA(9);
    MXA(s[1]) MXB(s[1], 1)
    { auto rr = __builtin_amdgcn_permlane32_swap(__float_as_uint(mx), __float_as_uint(mx), false, false);
      mx = __builtin_fmaxf(__uint_as_float(rr[0]), __uint_as_float(rr[1])); }
    f32x16 nm;
#pragma unroll
    for (int r = 0; r < 16; ++r) nm[r] = -mx;
    OPQ(nm);
    FA(10);
    if (!hl) { WAIT_V(0); FA(11); BAR(); }
    SBAR();
    FA(12);
    bf16x8 vf[2][2], vx0, vx1;
    u32x4 p[2][2], pd[2];
    f32x16 o = zero;
    f32x4 ls = f32x4{};
    float e[16];
    unsigned pc[8];
#define EDGE(j) if (__builtin_expect((j) == jmin - 1, 0)) { _Pragma("unroll") for (int r = 0; r < 16; ++r) nm[r] = -INFINITY; OPQ(nm); } SBAR();
#define MEET(n) if ((n) == X3P) { if (hl) { WAIT_V(0); BAR(); } SBAR(); }
#define EX(b, r) e[r] = __builtin_amdgcn_exp2f(s[b][r]); OPQ(e[r]);
#define CV(b, i) pc[i] = pk2(e[2 * (i)], e[2 * (i) + 1]); if (((i) & 3) == 3) { p[b][(i) >> 2] = (u32x4){pc[(i) & 4], pc[((i) & 4) + 1], pc[((i) & 4) + 2], pc[((i) & 4) + 3]}; OPQ(p[b][(i) >> 2]); }
#define RV(d0, d1, j) d0 = lds128(vb + (j) * 2048); d1 = lds128(vb + (j) * 2048 + 1024);
    RV(vf[0][0], vf[0][1], 8) RV(vx0, vx1, 0)
    EDGE(7)
    SA(s[1], 7, nm)
#pragma unroll
    for (int r = 0; r < 16; ++r) s[0][r] = sd[r] - mx;
    OPQ(s[0]);
    SBAR();
    SB(s[1], 7)
#pragma unroll
    for (int r = 0; r < 16; ++r) { EX(0, r) }
#pragma unroll
    for (int i = 0; i < 8; ++i) { CV(0, i) }
    pd[0] = p[0][0] & mk[0]; pd[1] = p[0][1] & mk[1];
    OPQ(pd[0]); OPQ(pd[1]);
    SBAR();
    MEET(0)
    FA(13);
    EDGE(6)
    SA(s[0], 6, nm) EX(1, 0) EX(1, 1) EX(1, 2) SBAR();
    SB(s[0], 6) EX(1, 3) EX(1, 4) EX(1, 5) SBAR();
    ls = M16(ones, asbf(p[0][0]), ls); SBAR(); EX(1, 6) SBAR();
    ls = M16(ones, asbf(p[0][1]), ls); SBAR(); p[0][0] ^= pd[0]; SBAR();
    o = M32(vf[0][0], asbf(pd[0]), o); SBAR(); p[0][1] ^= pd[1]; EX(1, 7) SBAR();
    o = M32(vf[0][1], asbf(pd[1]), o); SBAR(); EX(1, 8) EX(1, 9) EX(1, 10) RV(vf[1][0], vf[1][1], 7) SBAR();
    o = M32(vx0, asbf(p[0][0]), o); SBAR(); EX(1, 11) EX(1, 12) EX(1, 13) SBAR();
    o = M32(vx1, asbf(p[0][1]), o); SBAR(); EX(1, 14) EX(1, 15)
    CV(1, 0) CV(1, 1) CV(1, 2) CV(1, 3) CV(1, 4) CV(1, 5) CV(1, 6) CV(1, 7) SBAR();
    MEET(1)
#define ITER(n, b, c)                                                                                         \
    EDGE(7 - (n))                                                                                             \
    SA(s[c], 7 - (n), nm) EX(b, 0) EX(b, 1) EX(b, 2) SBAR();                                                  \
    SB(s[c], 7 - (n)) EX(b, 3) EX(b, 4) EX(b, 5) SBAR();                                                      \
    ITAIL(n, b, c)
#define ITAIL(n, b, c)                                                                                        \
    o = M32(vf[c][0], asbf(p[c][0]), o); SBAR(); EX(b, 6) EX(b, 7) EX(b, 8) SBAR();                           \
    o = M32(vf[c][1], asbf(p[c][1]), o); SBAR(); EX(b, 9) EX(b, 10) EX(b, 11) RV(vf[b][0], vf[b][1], 8 - (n)) SBAR(); \
    ls = M16(ones, asbf(p[c][0]), ls); SBAR(); EX(b, 12) EX(b, 13) CV(b, 0) CV(b, 1) CV(b, 2) SBAR();         \
    ls = M16(ones, asbf(p[c][1]), ls); SBAR(); EX(b, 14) EX(b, 15) CV(b, 3) CV(b, 4) CV(b, 5) CV(b, 6) CV(b, 7) SBAR(); \
    MEET(n)
    FA(14); ITER(2, 0, 1) FA(15); ITER(3, 1, 0) FA(16); ITER(4, 0, 1) FA(17); ITER(5, 1, 0) FA(18); ITER(6, 0, 1) FA(19);
    EX(1, 0) EX(1, 1) EX(1, 2) EX(1, 3) EX(1, 4) EX(1, 5) SBAR();
    ITAIL(7, 1, 0)
    FA(20);
    ls = M16(ones, asbf(p[1][0]), ls); ls = M16(ones, asbf(p[1][1]), ls);
    o = M32(vf[1][0], asbf(p[1][0]), o); o = M32(vf[1][1], asbf(p[1][1]), o);
    SBAR();
    FA(21);
    LANE();
    const float rl = __builtin_amdgcn_rcpf((g & 1) ? ls[1] : ls[0]);
#pragma unroll
    for (int r = 0; r < 16; ++r) o[r] *= rl;
    const int vo = (g & 1) * 1024 + (g >> 1) * 256 + (lane & 15) * 16, so = ((4 * m + tl) * 32 + 2 * hp + hl) * 2048;
    __builtin_amdgcn_raw_buffer_store_b128(pack8(o, 0), ro, vo, so, 16);
    __builtin_amdgcn_raw_buffer_store_b128(pack8(o, 1), ro, vo + 512, so, 16);
  }
  FA(22);
  FG(0);
  PRIO(0);
  LANE();
  gu64* of = (gu64*)(ws + WS_OF);
  gu64* fe = of + (m2 * 4 + tl) * 32 + ((8 * hl + (lane & 7) + r4) & 15);
  unsigned long long seen;
  if (!hl) {
    BAR();
    FG(1);
    ISSUE_WO(0, 1, L_WO);
    if (NA1 > 1) ISSUE_WO(2, 1, L_WO + 32 * KB);
    if (NA1 > 2) ISSUE_WO(1, 1, L_WO + 16 * KB);
    if (NA1 > 3) ISSUE_WO(3, 1, 0);
    FG(2);
    WAIT_V(4 * NA1);
  } else {
    WAIT_V(0);
  }
  if (lane == 0) __hip_atomic_store(of + ((m * 4 + tl) * 2 + hl) * 16 + hp, tag, RLX_AGENT);
  FG(3);
  FSOUT(23);
  STAMP(11);
  if (!hl) {
    if (NA1 <= 1) ISSUE_WO(2, 1, L_WO + 32 * KB);
    if (NA1 <= 2) ISSUE_WO(1, 1, L_WO + 16 * KB);
    if (NA1 <= 3) ISSUE_WO(3, 1, 0);
  }
  {
    FG(4);
    seen = __hip_atomic_load(fe, RLX_AGENT);
    for (unsigned spins = 0; !__all(seen == tag) && spins < SPIN_CAP; ++spins) seen = __hip_atomic_load(fe, RLX_AGENT);
    FG(5);
    STAMP(12);
    bf16x8 a[16];
    const int vl = lane * 16, so = (4 * m2 + tl) * 65536;
    const int sa0 = so + ((8 * hl + r4) & 15) * 4096, sa1 = so + ((8 * hl + 4 + r4) & 15) * 4096;
    auto loadA = [&](int odd) {
#pragma unroll
      for (int i = 0; i < 16; ++i)
        a[i] = asbf(__builtin_amdgcn_raw_buffer_load_b128(ro, vl + (i & 1) * 1024 + odd * 2048, (i < 8 ? sa0 : sa1) + ((i >> 1) & 3) * 4096, 16));
    };
    loadA(0);
    seen = __hip_atomic_load(fe + 16, RLX_AGENT);
    SBAR();
    FG(6);
    const int sw = 2 * (g & 1) + (q4 >> 1);
    const int bw = (16 * (g >> 1) + 4 * (g & 1) + q4) * 128 + 8 * pp + (sw << 5);
    LAS const char* pb[4];
    f32x4 c[2][4];
    s16x4 bl[2][4], bh[2][4];
#pragma unroll
    for (int j = 0; j < 4; ++j) { c[0][j] = f32x4{}; c[1][j] = f32x4{}; pb[j] = L + L_WE + hl * 32768 + (bw ^ (j << 5)); OPQ(pb[j]); }
#define RB(n, j, off) bl[n][j] = ldstr(pb[j] + (off)); bh[n][j] = ldstr(pb[j] + (off) + 1024);
#define MB(b, j, i) c[0][j] = M16(cat(bl[b][j], bh[b][j]), a[2 * (i)], c[0][j]); c[1][j] = M16(cat(bl[b][j], bh[b][j]), a[2 * (i) + 1], c[1][j]); SBAR();
#define HEAD(b, n, i, off) MB(b, 0, i) RB(n, 0, off) SBAR(); MB(b, 1, i) RB(n, 1, off) SBAR(); MB(b, 2, i) RB(n, 2, off) SBAR(); MB(b, 3, i) RB(n, 3, off) SBAR();
#define HEADL(b, i) MB(b, 0, i) MB(b, 1, i) MB(b, 2, i) MB(b, 3, i)
    RB(0, 0, 0) RB(0, 1, 0) RB(0, 2, 0) RB(0, 3, 0) SBAR();
    HEAD(0, 1, 0, 4096) FG(7); HEAD(1, 0, 1, 8192) HEAD(0, 1, 2, 12288) HEAD(1, 0, 3, 16384) FG(8);
    HEAD(0, 1, 4, 20480) HEAD(1, 0, 5, 24576) HEAD(0, 1, 6, 28672) HEADL(1, 7)
    FG(9);
    for (unsigned spins = 0; !__all(seen == tag) && spins < SPIN_CAP; ++spins) seen = __hip_atomic_load(fe + 16, RLX_AGENT);
    FG(10);
    STAMP(14);
    loadA(1);
    SBAR();
    FG(11);
    BAR();
    FG(12);
#pragma unroll
    for (int j = 0; j < 4; ++j) MOVE(pb[j], L_WO - L_WE);
    RB(0, 0, 0) RB(0, 1, 0) RB(0, 2, 0) RB(0, 3, 0) SBAR();
    HEAD(0, 1, 0, 4096) FG(13); HEAD(1, 0, 1, 8192) HEAD(0, 1, 2, 12288)
    {
      const int dlt = hl ? -(L_WO + 32768) : 16384;
#pragma unroll
      for (int j = 0; j < 4; ++j) MOVE(pb[j], dlt);
      SBAR();
    }
    HEAD(1, 0, 3, 0) FG(14);
    HEAD(0, 1, 4, 4096) HEAD(1, 0, 5, 8192) HEAD(0, 1, 6, 12288) HEADL(1, 7)
    FG(15);
    LAS char* xs = L + L_XCH + lane * 16;
#pragma unroll
    for (int j = 0; j < 4; ++j) *(LAS f32x4*)(xs + wid * 4096 + j * 1024) = hl ? c[0][j] : c[1][j];
    WAIT_L0(); FG(16); BAR(); FG(17);
    f32x4 r[4];
#pragma unroll
    for (int j = 0; j < 4; ++j) r[j] = (hl ? c[1][j] : c[0][j]) + *(LAS const f32x4*)(xs + (wid ^ 4) * 4096 + j * 1024);
    char* od = out + (size_t)(128 * m2 + 32 * tl + 16 * hl + (lane & 15)) * 2048 + nb * 128 + (g & 1) * 32 + (g >> 1) * 16;
#pragma unroll
    for (int j = 0; j < 4; j += 2) {
      auto r0 = __builtin_amdgcn_permlane16_swap(pk2(r[j][0], r[j][1]), pk2(r[j + 1][0], r[j + 1][1]), false, false);
      auto r1 = __builtin_amdgcn_permlane16_swap(pk2(r[j][2], r[j][3]), pk2(r[j + 1][2], r[j + 1][3]), false, false);
      *(u32x4*)(od + j * 32) = (u32x4){r0[0], r1[0], r0[1], r1[1]};
    }
    FG(18);
  }
  STAMP(16);
  if (bid == 0 && wid == 0) {
    gu64* f = (gu64*)(ws + WS_KVF) + lane * 4;
    for (unsigned spins = 0; spins < SPIN_CAP; ++spins) {
      const bool ok = __hip_atomic_load(f, RLX_AGENT) == tag && __hip_atomic_load(f + 256, RLX_AGENT) == tag &&
                      __hip_atomic_load(f + 512, RLX_AGENT) == tag && __hip_atomic_load(f + 768, RLX_AGENT) == tag;
      if (__all(ok)) break;
      __builtin_amdgcn_s_sleep(1);
    }
    if (lane == 0) __hip_atomic_store((gu64*)(ws + WS_E), tag, RLX_AGENT);
  }
  if (wid == MW && (MBID < 0 || bid == MBID)) {
    unsigned long long d = (mtb - mta) * MK; if (d > 20000) d = 20000;
    const unsigned long long t0 = __builtin_amdgcn_s_memrealtime();
    while (__builtin_amdgcn_s_memrealtime() - t0 < d) __builtin_amdgcn_s_sleep(1);
  }
}

__global__ void k_end() { asm volatile("kend_lbl:"); }

extern "C" void kernel_launch(void* const* d_in, const int* in_sizes, int n_in, void* d_out, int out_size, void* d_ws,
                              size_t ws_size, hipStream_t stream) {
  (void)hipFuncSetAttribute((const void*)k_fused, hipFuncAttributeMaxDynamicSharedMemorySize, L_ALL);
  k_fused<<<256, 512, L_ALL, stream>>>((const char*)d_in[0], (const char*)d_in[1], (const char*)d_in[2], (char*)d_ws, (char*)d_out);
}
```

```cpp
#define OST 1
#include <hip/hip_runtime.h>
#include <hip/hip_bf16.h>
#include <utility>

typedef short bf16x8 __attribute__((ext_vector_type(8)));
typedef short s16x2 __attribute__((ext_vector_type(2)));
typedef short s16x4 __attribute__((ext_vector_type(4)));
typedef float f32x2 __attribute__((ext_vector_type(2)));
typedef float f32x4 __attribute__((ext_vector_type(4)));
typedef float f32x16 __attribute__((ext_vector_type(16)));
typedef unsigned u32x4 __attribute__((ext_vector_type(4)));
typedef __bf16 bf2_t __attribute__((ext_vector_type(2)));
typedef __amdgpu_buffer_rsrc_t rsrc_t;
#define LAS __attribute__((address_space(3)))
#define GAS __attribute__((address_space(1)))
typedef GAS unsigned long long gu64;
#define DI __device__ __forceinline__
#define M32(a, b, c) __builtin_amdgcn_mfma_f32_32x32x16_bf16((a), (b), (c), 0, 0, 0)
#define M16(a, b, c) __builtin_amdgcn_mfma_f32_16x16x32_bf16((a), (b), (c), 0, 0, 0)
#define SBAR() __builtin_amdgcn_sched_barrier(0)
#define BAR() asm volatile("s_barrier" ::: "memory")
#define WAIT_V(n) asm volatile("s_waitcnt vmcnt(%0)" ::"n"(n) : "memory")
#define WAIT_VL(n) asm volatile("s_waitcnt vmcnt(%0) lgkmcnt(0)" ::"n"(n) : "memory")
#define WAIT_L0() asm volatile("s_waitcnt lgkmcnt(0)" ::: "memory")
#define LGKM0() __builtin_amdgcn_s_waitcnt(0xc07f)
#define VM0() __builtin_amdgcn_s_waitcnt(0x0f70)
#define PRIO(n) __builtin_amdgcn_s_setprio(n)
#define RLX_AGENT __ATOMIC_RELAXED, __HIP_MEMORY_SCOPE_AGENT

#define STAMP(i)
#define TSV(slot)
#define FS(i)
#define FSOUT(n)
#define ROT 2
#define X3P 2
#define FSSET 0
#define CPF 1
#define NA1 2

constexpr int SEQ = 2048, DM = 1024, KB = 1024;
constexpr int xslot(int t) { return 32 * KB + 16 * KB * (t % 6); }
constexpr int wslot(int t) { return t % 3 == 0 ? 0 : t % 3 == 1 ? 144 * KB : 16 * KB; }
constexpr int pslot(int s) { return s == 0 || s == 4 ? 128 * KB : s == 1 || s == 7 ? 144 * KB : s == 2 ? 16 * KB : s == 3 ? 0 : s == 5 ? 96 * KB : 112 * KB; }
constexpr int L_V = 0, L_K = 48 * KB, L_Q = 128 * KB, L_WE = 96 * KB, L_WO = 48 * KB, L_XCH = 96 * KB, L_ALL = 160 * KB;
constexpr int WROW = 6144, WTILE = 64 * WROW;
constexpr size_t WS_E = 0, WS_KVF = 4096, WS_OF = 16384, WS_KV = 1u << 20, WS_O = 16u << 20;
constexpr float QSCALE = 0.17677669529663687f * 1.4426950408889634f;
constexpr unsigned SPIN_CAP = 1u << 18;

template <class F, int... I> DI void sfor_(F&& f, std::integer_sequence<int, I...>) { (f(std::integral_constant<int, I>{}), ...); }
template <int N, class F> DI void sfor(F&& f) { sfor_(f, std::make_integer_sequence<int, N>{}); }

DI unsigned pk2(float a, float b) { f32x2 f = {a, b}; return __builtin_bit_cast(unsigned, __builtin_convertvector(f, bf2_t)); }
DI u32x4 pack44(const f32x4& a, const f32x4& b) { return (u32x4){pk2(a[0], a[1]), pk2(a[2], a[3]), pk2(b[0], b[1]), pk2(b[2], b[3])}; }
DI u32x4 pack8(const f32x16& x, int s) {
  return (u32x4){pk2(x[8 * s], x[8 * s + 1]), pk2(x[8 * s + 2], x[8 * s + 3]), pk2(x[8 * s + 4], x[8 * s + 5]), pk2(x[8 * s + 6], x[8 * s + 7])};
}
DI bf16x8 asbf(u32x4 v) { return __builtin_bit_cast(bf16x8, v); }
DI bf16x8 lds128(LAS const char* p) { return *(LAS const bf16x8*)p; }
DI s16x4 ldstr(LAS const char* p) { return __builtin_amdgcn_ds_read_tr16_b64_v4i16((LAS s16x4*)p); }
DI bf16x8 cat(s16x4 a, s16x4 b) { return __builtin_shufflevector(a, b, 0, 1, 2, 3, 4, 5, 6, 7); }
#define DMA4_(AUX)                                                                                             \
  asm volatile("s_add_u32 m0, %0, %1\n\ts_nop 0\n\t"                                                           \
               "buffer_load_dwordx4 %2, %4, %5 offen" AUX " lds\n\t"                                           \
               "buffer_load_dwordx4 %3, %4, %5 offen offset:1024" AUX " lds\n\t"                               \
               "buffer_load_dwordx4 %2, %4, %6 offen offset:2048" AUX " lds\n\t"                               \
               "buffer_load_dwordx4 %3, %4, %6 offen offset:3072" AUX " lds"                                   \
               ::"s"(base), "n"(OFF), "v"(v0), "v"(v1), "s"(r), "s"(s0), "s"(s1) : "memory", "scc")
template <int OFF> DI void dma4(rsrc_t r, unsigned base, int v0, int v1, int s0, int s1) { DMA4_(""); }
template <int OFF> DI void dma4c(rsrc_t r, unsigned base, int v0, int v1, int s0, int s1) { DMA4_(" sc1"); }
DI unsigned long long read_tag(const char* ws) {
  unsigned long long e;
  asm volatile("s_load_dwordx2 %0, %1, 0x0\n\ts_waitcnt lgkmcnt(0)" : "=s"(e) : "s"(ws) : "memory");
  e = e * 0x9E3779B97F4A7C15ull + 0x632BE59BD9B4E019ull;
  asm volatile("" : "+s"(e));
  return e;
}
#define MX3(a, b, c) __builtin_fmaxf(__builtin_fmaxf((a), (b)), (c))
constexpr unsigned long long diag_mask(int r) {
  const int k0 = (r & 3) + 8 * (r >> 3) + 16 * ((r >> 2) & 1);
  return (unsigned long long)(0xffffffffu << k0) | ((unsigned long long)(0xffffffffu << (k0 + 4)) << 32);
}
#define IC(n) std::integral_constant<int, n>{}
#define OPQ(p) asm volatile("" : "+v"(p))
#define MOVE(p, d) do { p += (d); OPQ(p); } while (0)
#define LANE() int lane = threadIdx.x & 63; OPQ(lane); const int g = lane >> 4, q4 = (lane & 15) >> 2, pp = lane & 3; (void)g; (void)q4; (void)pp
#define FA(i) FS(i)
#define FG(i)
#define FP(i)

__global__ __launch_bounds__(512, 2) void k_fused(const char* __restrict__ x, const char* __restrict__ w, const char* __restrict__ wo,
                                                  char* __restrict__ ws, char* __restrict__ out) {
  extern __shared__ __attribute__((aligned(16))) char lds_[];
  LAS char* L = (LAS char*)lds_;
  const unsigned lbase = (unsigned)(size_t)L;
  unsigned long long pc0;
  asm volatile("s_getpc_b64 %0\n\tkstart_lbl:" : "=s"(pc0));
  asm volatile("" ::"s"(x), "s"(w), "s"(wo), "s"(ws), "s"(out));
  const int bid = blockIdx.x, xcd = bid & 7, idx = bid >> 3;
  const int m = (xcd & 1) * 8 + (idx & 7), hp = (xcd >> 1) * 4 + (idx >> 3);
  const int wid = __builtin_amdgcn_readfirstlane(threadIdx.x >> 6), w4 = wid & 3;
  const int kc = idx + (idx >> 3), kg = idx & 4;
  auto ktile = [&](int t) {
    if (!ROT) return t;
    const int a1 = t < 2 || t >= 14 ? 0 : (t + 2) & 12, b1 = t < 2 || t >= 14 ? t : t + 2;
    return ROT == 1 ? (t + 4 * kc) & 15 : (kg ? a1 : t & 12) + (((kg ? b1 : t) + kc) & 3);
  };
  const rsrc_t rw = __builtin_amdgcn_make_buffer_rsrc((void*)(w - 2048), 0, DM * 3 * DM * 2 + 2048, 0x00020000);
  const unsigned lb4 = lbase + w4 * 4096;
  unsigned long long tag;
  int vQ0, vQ1;
  auto issueP = [&](auto sc) {
    constexpr int s = sc;
    dma4<pslot(s)>(rw, lb4, vQ0, vQ1, ktile((10 + 2 * s) & 15) * WTILE + 2048, ktile((11 + 2 * s) & 15) * WTILE);
  };
  const int hl = wid >> 2, tl = w4;
  LAS const char *qs, *vb, *kb;
  u32x4 mk[2];
  bf16x8 ones;
  unsigned long long dm[16];
  auto prep = [&]() {
    LANE();
    qs = L + L_Q + hl * 8192 + tl * 2048 + lane * 16;
    vb = L + L_V + hl * 24576 + tl * 2048 + lane * 16;
    kb = vb + L_K;
    const short dq = (short)((lane & 31) - 4 * (lane >> 5));
    const s16x2 d2 = {dq, dq};
#pragma unroll
    for (int i = 0; i < 8; ++i) {
      const short kk = (short)(((2 * i) & 3) + 8 * (i >> 2) + 16 * ((i >> 1) & 1));
      mk[i >> 2][i & 3] = __builtin_bit_cast(unsigned, (s16x2)(((s16x2){(short)(kk - 1), kk} - d2) >> 15));
    }
    const short one = (((lane >> 4) ^ lane) & 1) ? (short)0 : (short)0x3F80;
    ones = (bf16x8){one, one, one, one, one, one, one, one};
    asm volatile("" : "+v"(mk[0]), "+v"(mk[1]), "+v"(ones), "+v"(qs), "+v"(vb), "+v"(kb));
#pragma unroll
    for (int r = 0; r < 16; ++r) { dm[r] = diag_mask(r); asm volatile("" : "+s"(dm[r])); }
  };
  STAMP(0);
  if (__builtin_expect(wid >= 4, 1)) {
    PRIO(2);
    LANE();
    const rsrc_t rx = __builtin_amdgcn_make_buffer_rsrc((void*)x, 0, SEQ * DM * 2, 0x00020000);
    const int vX0 = (m * 128 + 32 * w4 + (lane >> 3)) * 2048 + (((lane & 7) ^ (lane >> 4)) << 4), vX1 = (vX0 ^ 64) + 16384 - 1024;
    auto issueX = [&](auto tc) { constexpr int t = tc; const int s0 = ktile(t) * 128; dma4<xslot(t)>(rx, lb4, vX0, vX1, s0, s0 + 32768 - 2048); };
    issueX(IC(0));
    SBAR();
    const int vA = (16 * w4 + (lane >> 3)) * WROW + hp * 128 + (((lane & 7) ^ (((lane >> 4) & 1) << 1)) << 4);
    const int vB = (vA ^ 64) + 8 * WROW - 1024;
    auto issueW = [&](auto tc) { constexpr int t = tc; const int s0 = ktile(t) * WTILE + 4096; dma4<wslot(t)>(rw, lb4, vA, vB, s0, s0); };
    issueW(IC(0)); issueX(IC(1)); issueW(IC(1));
    SBAR();
    tag = read_tag(ws);
    LAS const char* px[4];
#pragma unroll
    for (int s = 0; s < 4; ++s) { px[s] = L + 32 * KB + (32 * w4 + (lane & 31)) * 128 + (((2 * s + (lane >> 5)) ^ ((lane >> 1) & 7)) << 4); OPQ(px[s]); }
#define XO(t) (16 * KB * ((t) % 6 < 4 ? (t) % 6 : (t) % 6 - 4))
#define MOVEX(d) do { MOVE(px[0], d); MOVE(px[1], d); MOVE(px[2], d); MOVE(px[3], d); } while (0)
    bf16x8 xh[10][4];
    WAIT_V(8); BAR();
    STAMP(1);
    sfor<14>([&](auto tc) {
      constexpr int t = tc;
      issueX(IC(t + 2)); issueW(IC(t + 2));
      if constexpr (t == 13) issueP(IC(0));
      if constexpr (t == 4 || t == 10) MOVEX(64 * KB);
      if constexpr (t == 6) MOVEX(-64 * KB);
      if constexpr (t == 12) { vQ0 = vA ^ (((lane >> 4) & 1) * 0x60); vQ1 = vQ0 + 8 * WROW - 1024; }
      if constexpr (t < 10) {
#pragma unroll
        for (int s = 0; s < 4; ++s) xh[t][s] = lds128(px[s] + XO(t));
      }
      SBAR();
      WAIT_VL(t == 13 ? 12 : 8); TSV(t + 1); BAR();
    });
    issueP(IC(1)); WAIT_V(8); TSV(15); BAR();
    issueP(IC(2));
    bf16x8 xl[3];
    s16x4 fl[6], fh[6];
    f32x16 aq[2];
    aq[0] = f32x16{}; aq[1] = f32x16{}; OPQ(aq[0]); OPQ(aq[1]);
    xl[0] = lds128(px[0] + XO(10)); xl[1] = lds128(px[1] + XO(10));
    LAS const char* pq[2];
    pq[0] = L + 96 * KB + (8 * (g >> 1) + q4) * 128 + (((g & 1) ^ (q4 & 2)) << 5) + 8 * pp;
    pq[1] = L + 96 * KB + (8 * (g >> 1) + q4) * 128 + (((g & 1) ^ (q4 & 2) ^ 2) << 5) + 8 * pp;
    OPQ(pq[0]); OPQ(pq[1]);
#define PO(n) (pslot((n) >> 4) % (96 * KB) + (((n) >> 3) & 1) * 2048 + (((n) >> 1) & 3) * 4096)
    SBAR();
    WAIT_V(8); TSV(16); BAR();
    PRIO(0);
    STAMP(2);
    FP(0);
    auto rdw = [&](auto nc) { constexpr int n = nc; fl[n % 6] = ldstr(pq[n & 1] + PO(n)); fh[n % 6] = ldstr(pq[n & 1] + PO(n) + 512); };
    auto rdx = [&](auto sc) { constexpr int st = sc; xl[st % 3] = lds128(px[st & 3] + XO(10 + (st >> 2))); };
    sfor<5>([&](auto nc) { rdw(nc); SBAR(); });
    sfor<128>([&](auto nc) {
      constexpr int n = nc, u = n >> 3, st = n >> 1, h = n & 1;
      constexpr int nb = (n | 15) + 1, d = nb - n;
      constexpr bool cut = nb <= 80;
      constexpr int r0 = !cut || d > 5 ? n + 5 : d == 3 ? nb : d == 2 ? nb + 2 : d == 1 ? nb + 4 : 999;
      constexpr int r1 = cut && (d == 3 || d == 2) ? r0 + 1 : 999;
      aq[h] = M32(cat(fl[n % 6], fh[n % 6]), u < 6 ? xl[st % 3] : xh[u < 6 ? 0 : u - 6][st & 3], aq[h]); SBAR();
      if constexpr (r0 < 128) rdw(IC(r0));
      if constexpr (r1 < 128) rdw(IC(r1));
      if constexpr (n == 11) MOVEX(-64 * KB);
      if constexpr (n == 27 || n == 59) { MOVE(pq[0], n == 27 ? -96 * KB : 96 * KB); MOVE(pq[1], n == 27 ? -96 * KB : 96 * KB); }
      if constexpr (cut && d == 4) {
        SBAR();
        if constexpr (nb == 16) WAIT_V(4);
        if constexpr (nb == 32) WAIT_V(0);
        LGKM0(); SBAR(); FP(2 * (nb / 16) - 1); BAR(); FP(2 * (nb / 16)); SBAR();
      }
      if constexpr (h == 0 && st + 2 < 24) rdx(IC(st + 2));
      SBAR();
    });
    FP(11);
    STAMP(3);
    LAS char* dq = L + L_Q + w4 * 2048 + lane * 16;
#pragma unroll
    for (int h = 0; h < 2; ++h) {
      aq[h] *= QSCALE;
#pragma unroll
      for (int c = 0; c < 2; ++c)
        *(LAS u32x4*)(dq + h * 8192 + c * 1024) = (u32x4){pk2(aq[h][4 * c], aq[h][4 * c + 1]), pk2(aq[h][4 * c + 2], aq[h][4 * c + 3]),
                                                          pk2(aq[h][8 + 4 * c], aq[h][9 + 4 * c]), pk2(aq[h][10 + 4 * c], aq[h][11 + 4 * c])};
    }
    WAIT_L0();
    FP(12);
  } else {
    LANE();
    const int wm = wid & 1, wn = wid >> 1;
    unsigned pf = 0;
    if (wid == 0) {
      const unsigned o = (unsigned)(lane * 32 + idx) * 128u;
      unsigned long long keep;
      asm volatile("v_cmp_gt_u32 vcc, kend_lbl - kstart_lbl, %3\n\ts_and_saveexec_b64 %1, vcc\n\t"
                   "global_load_dword %0, %3, %2\n\ts_mov_b64 exec, %1"
                   : "+v"(pf), "=&s"(keep) : "s"(pc0), "v"(o) : "vcc", "scc", "memory");
    }
    tag = read_tag(ws);
    const int ax0 = (64 * wm + (lane & 15)) * 128 + ((g ^ ((lane >> 1) & 7)) << 4), ax1 = ax0 ^ 64;
    const int aw0 = (g >> 1) * 4096 + (8 * (g & 1) + q4) * 128 + (((2 * wn) ^ ((q4 >> 1) & 1) ^ ((g & 1) << 1)) << 5) + 8 * pp, aw1 = aw0 ^ 32;
    vQ0 = (16 * w4 + (lane >> 3)) * WROW + hp * 128 + (((lane & 7) ^ (((lane >> 4) & 1) << 2)) << 4); vQ1 = vQ0 + 8 * WROW - 1024;
    int ok = (g >> 1) * 1024 + (16 * ((lane >> 3) & 1) + (lane & 7) + 32 * (g & 1)) * 16, ov = (g >> 1) * 1024 + ((lane & 15) + 32 * (g & 1)) * 16;
    asm volatile("" : "+v"(vQ0), "+v"(vQ1), "+v"(ok), "+v"(ov));
    f32x4 acc[4][4];
#pragma unroll
    for (int i = 0; i < 4; ++i)
#pragma unroll
      for (int j = 0; j < 4; ++j) { acc[i][j] = f32x4{}; OPQ(acc[i][j]); }
    bf16x8 xf[2][4];
    s16x4 wl[2][4], wh[2][4];
#define WF(b, j) cat(wl[b][j], wh[b][j])
#define RX(n, i) xf[n][i] = lds128(px + (i) * 2048)
#define RL(n, j, s) wl[n][j] = ldstr(((j) & 1 ? pw1 : pw0) + ((j) >> 1) * 2048 + (s) * 8192)
#define RH(n, j, s) wh[n][j] = ldstr(((j) & 1 ? pw1 : pw0) + ((j) >> 1) * 2048 + (s) * 8192 + 512)
#define MM(b, i, j) acc[i][j] = ((j) < 2) ? M16(WF(b, j), xf[b][i], acc[i][j]) : M16(xf[b][i], WF(b, j), acc[i][j])
#define G(mm, rd) mm; SBAR(); rd; SBAR();
#define STEP1(b, n, s)                                                                                        \
  G(MM(b, 0, 0), RX(n, 0)) G(MM(b, 1, 0), RL(n, 0, s)) G(MM(b, 2, 0), RH(n, 0, s)) G(MM(b, 3, 0), RX(n, 1))   \
  G(MM(b, 0, 1), RX(n, 2)) G(MM(b, 1, 1), RX(n, 3)) G(MM(b, 2, 1), RL(n, 1, s)) G(MM(b, 3, 1), RH(n, 1, s))   \
  G(MM(b, 0, 2), RL(n, 2, s)) G(MM(b, 1, 2), RH(n, 2, s)) G(MM(b, 2, 2), RL(n, 3, s)) G(MM(b, 3, 2), RH(n, 3, s))
#define TAIL1(b) MM(b, 0, 3); MM(b, 1, 3); MM(b, 2, 3); MM(b, 3, 3); SBAR(); LGKM0(); SBAR();
    int xs = xslot(0), wsl = wslot(0);
    LAS const char* px = L + xs + ax0;
    LAS const char* pw0 = L + wsl + aw0;
    LAS const char* pw1 = L + wsl + aw1;
    BAR();
    STAMP(1);
    RX(0, 0); RL(0, 0, 0); RH(0, 0, 0); RX(0, 1); RX(0, 2); RX(0, 3); RL(0, 1, 0); RH(0, 1, 0); RL(0, 2, 0); RH(0, 2, 0); RL(0, 3, 0); RH(0, 3, 0);
    px = L + xs + ax1;
    LGKM0(); SBAR();
#pragma unroll 1
    for (int t = 0; t < 15; ++t) {
      STEP1(0, 1, 1)
      xs += 16 * KB; if (xs == 128 * KB) xs = 32 * KB;
      wsl = wsl == 0 ? 144 * KB : wsl == 16 * KB ? 0 : 16 * KB;
      px = L + xs + ax0; pw0 = L + wsl + aw0; pw1 = L + wsl + aw1;
      TAIL1(0)
      asm volatile("s_waitcnt vmcnt(0)" ::"v"(pf) : "memory");
      TSV(t + 1); BAR(); SBAR();
      STEP1(1, 0, 0)
      px = L + xs + ax1;
      TAIL1(1)
    }
    STEP1(0, 1, 1) TAIL1(0)
    TSV(16); BAR(); SBAR();
    PRIO(2);
    STAMP(2);
    FP(0);
    MM(1, 0, 0); MM(1, 0, 1); MM(1, 1, 0); MM(1, 1, 1); SBAR();
    issueP(IC(3)); SBAR();
    MM(1, 2, 0); MM(1, 2, 1); MM(1, 3, 0); MM(1, 3, 1);
    MM(1, 0, 2); MM(1, 1, 2); MM(1, 0, 3); MM(1, 1, 3); MM(1, 2, 2); MM(1, 3, 2); MM(1, 2, 3); MM(1, 3, 3);
    const rsrc_t rkv = __builtin_amdgcn_make_buffer_rsrc((void*)(ws + WS_KV), 0, 8u << 20, 0x00020000);
    const int dg = ((2 * hp + wn) * 64 + 4 * m + 2 * wm) * 4096;
    u32x4 kp[4], vp[4];
#pragma unroll
    for (int i = 0; i < 4; ++i) {
      kp[i] = pack44(acc[i][0], acc[i][1]);
      __builtin_amdgcn_raw_buffer_store_b128(kp[i], rkv, ok + (i & 1) * 128, dg + (i >> 1) * 4096, 16);
    }
#pragma unroll
    for (int i = 0; i < 4; ++i) {
      vp[i] = pack44(acc[(i >> 1) * 2][2 + (i & 1)], acc[(i >> 1) * 2 + 1][2 + (i & 1)]);
      __builtin_amdgcn_raw_buffer_store_b128(vp[i], rkv, ov + 2048 + (i & 1) * 256, dg + (i >> 1) * 4096, 16);
    }
    SBAR();
    FP(1); BAR(); FP(2);
    issueP(IC(4)); issueP(IC(5)); issueP(IC(6));
    FP(3); BAR(); FP(4);
    issueP(IC(7));
    prep();
    WAIT_V(24); FP(5); BAR(); FP(6);
    LAS char* dl = L + wn * 24576 + (8 + 2 * wm) * 2048;
#pragma unroll
    for (int i = 0; i < 4; ++i) *(LAS u32x4*)(dl + L_K + ok + (i >> 1) * 2048 + (i & 1) * 128) = kp[i];
    gu64* kvf = (gu64*)(ws + WS_KVF) + hp * 64;
    const int mb = max(m - 2 + wm, 0);
    gu64* fsrc = kvf + mb * 4 + wn * 2 + (lane & 1);
    WAIT_V(12);
    if (lane == 0) __hip_atomic_store(kvf + m * 4 + wid, tag, RLX_AGENT);
    FP(7); BAR(); FP(8);
    unsigned long long seen = __hip_atomic_load(fsrc, RLX_AGENT);
#pragma unroll
    for (int i = 0; i < 4; ++i) *(LAS u32x4*)(dl + L_V + ov + (i >> 1) * 2048 + (i & 1) * 256) = vp[i];
    WAIT_V(2); FP(9); BAR(); FP(10);
    for (unsigned spins = 0; !__all(seen == tag) && spins < SPIN_CAP; ++spins) seen = __hip_atomic_load(fsrc, RLX_AGENT);
    VM0();
    FP(11);
    STAMP(5);
    const int src = ((2 * hp + wn) * 64 + 4 * mb) * 4096, vl = lane * 16;
    const unsigned dr = lbase + wn * 24576 + wm * 8192;
    dma4c<L_K>(rkv, dr, vl, vl, src, src + 2048); dma4c<L_K + 4096>(rkv, dr, vl, vl, src + 8192, src + 10240);
    dma4c<L_V>(rkv, dr, vl, vl, src + 2048, src + 4096); dma4c<L_V + 4096>(rkv, dr, vl, vl, src + 10240, src + 12288);
    WAIT_VL(8);
    FP(12);
  }
  STAMP(6);
  BAR();
  STAMP(7);
  FA(0);
  if (hl) prep();
  const int m2 = 4 * (xcd & 3) + (idx & 3), nb = 8 * (xcd >> 2) + (idx >> 2), r4 = 4 * (idx & 3);
  const rsrc_t rwo = __builtin_amdgcn_make_buffer_rsrc((void*)wo, 0, DM * DM * 2, 0x00020000);
  const rsrc_t ro = __builtin_amdgcn_make_buffer_rsrc((void*)(ws + WS_O), 0, 4u << 20, 0x00020000);
#define ISSUE_WO(c, odd, dst) do { LANE(); const int v0_ = (lane >> 3) * 2048 + nb * 128 + (((lane & 7) ^ (((lane >> 4) & 3) << 1)) << 4); \
    const int s_ = (2 * ((4 * (c) + w4 + r4) & 15) + (odd)) * 65536; dma4<dst>(rwo, lb4, v0_, v0_ + 16384 - 1024, s_, s_ + 32768 - 2048); } while (0)
  {
    SBAR();
    const bf16x8 q0 = lds128(qs);
    bf16x8 kf[9][2];
#define RK(j) kf[j][0] = lds128(kb + (j) * 2048); kf[j][1] = lds128(kb + (j) * 2048 + 1024); SBAR();
    kf[8][0] = lds128(kb + 8 * 2048);
    const bf16x8 q1 = lds128(qs + 1024);
    kf[8][1] = lds128(kb + 8 * 2048 + 1024);
    SBAR();
    RK(0) RK(7)
    const int jmin = 8 - (4 * m + tl);
    const f32x16 zero = f32x16{};
    f32x16 s[2], sd;
    float mx, ma, mb_;
#define SA(d, j, c) d = M32(kf[j][0], q0, c); SBAR();
#define SB(d, j) d = M32(kf[j][1], q1, d); SBAR();
#define MXA(p) ma = MX3(p[0], p[1], p[2]); mb_ = MX3(p[3], p[4], p[5]); ma = MX3(ma, p[6], p[7]); mb_ = MX3(mb_, p[8], p[9]); SBAR();
#define MXB(p, j) ma = MX3(ma, p[10], p[11]); mb_ = MX3(mb_, p[12], p[13]); ma = MX3(ma, p[14], p[15]); \
    { unsigned pn_ = (j) >= jmin ? 0u : 0xff800000u; asm volatile("" : "+s"(pn_)); const float pen_ = __uint_as_float(pn_); \
      mx = MX3(mx, ma + pen_, mb_ + pen_); } SBAR();
#define MERGE(a, b) _Pragma("unroll") for (int r = (a); r < (b); ++r) sd[r] = __builtin_amdgcn_inverse_ballot_w64(dm[r]) ? sd[r] : s[0][r]; SBAR();
    FA(1);
    SA(sd, 8, zero) FA(2); RK(6) SB(sd, 8) RK(5) SA(s[0], 0, zero) RK(4) SB(s[0], 0) RK(3)
    SA(s[1], 7, zero) RK(2)
    if (__builtin_expect(jmin > 0, 0)) {
#pragma unroll
      for (int r = 0; r < 16; ++r) s[0][r] = -INFINITY;
      OPQ(s[0]);
    }
    SBAR();
    FA(3);
    SB(s[1], 7) RK(1) MERGE(0, 16)
    OPQ(sd);
    FA(4);
    if (hl) { ISSUE_WO(0, 0, L_WE); WAIT_L0(); BAR(); ISSUE_WO(2, 0, L_WE + 32 * KB); }
    SBAR();
    FA(5);
    SA(s[0], 6, zero) MXA(sd) SB(s[0], 6)
    ma = MX3(ma, sd[10], sd[11]); mb_ = MX3(mb_, sd[12], sd[13]); ma = MX3(ma, sd[14], sd[15]); mx = __builtin_fmaxf(ma, mb_); SBAR();
    MXA(s[1]) MXB(s[1], 7)
    FA(6);
    SA(s[1], 5, zero) MXA(s[0]) SB(s[1], 5) MXB(s[0], 6)
    SA(s[0], 4, zero) MXA(s[1]) SB(s[0], 4) MXB(s[1], 5)
    OPQ(mx);
    if (hl) ISSUE_WO(1, 0, L_WE + 16 * KB);
    SBAR();
    FA(7);
    SA(s[1], 3, zero) MXA(s[0]) SB(s[1], 3) MXB(s[0], 4)
    SA(s[0], 2, zero) MXA(s[1]) SB(s[0], 2) MXB(s[1], 3)
    OPQ(mx);
    if (hl) ISSUE_WO(3, 0, L_WE + 48 * KB);
    SBAR();
    FA(8);
    SA(s[1], 1, zero) MXA(s[0]) SB(s[1], 1) MXB(s[0], 2)
    FA(9);
    MXA(s[1]) MXB(s[1], 1)
    { auto rr = __builtin_amdgcn_permlane32_swap(__float_as_uint(mx), __float_as_uint(mx), false, false);
      mx = __builtin_fmaxf(__uint_as_float(rr[0]), __uint_as_float(rr[1])); }
    f32x16 nm;
#pragma unroll
    for (int r = 0; r < 16; ++r) nm[r] = -mx;
    OPQ(nm);
    FA(10);
    if (!hl) { WAIT_V(0); FA(11); BAR(); }
    SBAR();
    FA(12);
    bf16x8 vf[2][2], vx0, vx1;
    u32x4 p[2][2], pd[2];
    f32x16 o = zero;
    f32x4 ls = f32x4{};
    float e[16];
    unsigned pc[8];
#define EDGE(j) if (__builtin_expect((j) == jmin - 1, 0)) { _Pragma("unroll") for (int r = 0; r < 16; ++r) nm[r] = -INFINITY; OPQ(nm); } SBAR();
#define MEET(n) if ((n) == X3P) { if (hl) { WAIT_V(0); BAR(); } SBAR(); }
#define EX(b, r) e[r] = __builtin_amdgcn_exp2f(s[b][r]); OPQ(e[r]);
#define CV(b, i) pc[i] = pk2(e[2 * (i)], e[2 * (i) + 1]); if (((i) & 3) == 3) { p[b][(i) >> 2] = (u32x4){pc[(i) & 4], pc[((i) & 4) + 1], pc[((i) & 4) + 2], pc[((i) & 4) + 3]}; OPQ(p[b][(i) >> 2]); }
#define RV(d0, d1, j) d0 = lds128(vb + (j) * 2048); d1 = lds128(vb + (j) * 2048 + 1024);
    RV(vf[0][0], vf[0][1], 8) RV(vx0, vx1, 0)
    EDGE(7)
    SA(s[1], 7, nm)
#pragma unroll
    for (int r = 0; r < 16; ++r) s[0][r] = sd[r] - mx;
    OPQ(s[0]);
    SBAR();
    SB(s[1], 7)
#pragma unroll
    for (int r = 0; r < 16; ++r) { EX(0, r) }
#pragma unroll
    for (int i = 0; i < 8; ++i) { CV(0, i) }
    pd[0] = p[0][0] & mk[0]; pd[1] = p[0][1] & mk[1];
    OPQ(pd[0]); OPQ(pd[1]);
    SBAR();
    MEET(0)
    FA(13);
    EDGE(6)
    SA(s[0], 6, nm) EX(1, 0) EX(1, 1) EX(1, 2) SBAR();
    SB(s[0], 6) EX(1, 3) EX(1, 4) EX(1, 5) SBAR();
    ls = M16(ones, asbf(p[0][0]), ls); SBAR(); EX(1, 6) SBAR();
    ls = M16(ones, asbf(p[0][1]), ls); SBAR(); p[0][0] ^= pd[0]; SBAR();
    o = M32(vf[0][0], asbf(pd[0]), o); SBAR(); p[0][1] ^= pd[1]; EX(1, 7) SBAR();
    o = M32(vf[0][1], asbf(pd[1]), o); SBAR(); EX(1, 8) EX(1, 9) EX(1, 10) RV(vf[1][0], vf[1][1], 7) SBAR();
    o = M32(vx0, asbf(p[0][0]), o); SBAR(); EX(1, 11) EX(1, 12) EX(1, 13) SBAR();
    o = M32(vx1, asbf(p[0][1]), o); SBAR(); EX(1, 14) EX(1, 15)
    CV(1, 0) CV(1, 1) CV(1, 2) CV(1, 3) CV(1, 4) CV(1, 5) CV(1, 6) CV(1, 7) SBAR();
    MEET(1)
#define ITER(n, b, c)                                                                                         \
    EDGE(7 - (n))                                                                                             \
    SA(s[c], 7 - (n), nm) EX(b, 0) EX(b, 1) EX(b, 2) SBAR();                                                  \
    SB(s[c], 7 - (n)) EX(b, 3) EX(b, 4) EX(b, 5) SBAR();                                                      \
    ITAIL(n, b, c)
#define ITAIL(n, b, c)                                                                                        \
    o = M32(vf[c][0], asbf(p[c][0]), o); SBAR(); EX(b, 6) EX(b, 7) EX(b, 8) SBAR();                           \
    o = M32(vf[c][1], asbf(p[c][1]), o); SBAR(); EX(b, 9) EX(b, 10) EX(b, 11) RV(vf[b][0], vf[b][1], 8 - (n)) SBAR(); \
    ls = M16(ones, asbf(p[c][0]), ls); SBAR(); EX(b, 12) EX(b, 13) CV(b, 0) CV(b, 1) CV(b, 2) SBAR();         \
    ls = M16(ones, asbf(p[c][1]), ls); SBAR(); EX(b, 14) EX(b, 15) CV(b, 3) CV(b, 4) CV(b, 5) CV(b, 6) CV(b, 7) SBAR(); \
    MEET(n)
    FA(14); ITER(2, 0, 1) FA(15); ITER(3, 1, 0) FA(16); ITER(4, 0, 1) FA(17); ITER(5, 1, 0) FA(18); ITER(6, 0, 1) FA(19);
    EX(1, 0) EX(1, 1) EX(1, 2) EX(1, 3) EX(1, 4) EX(1, 5) SBAR();
    ITAIL(7, 1, 0)
    FA(20);
    ls = M16(ones, asbf(p[1][0]), ls); ls = M16(ones, asbf(p[1][1]), ls);
    o = M32(vf[1][0], asbf(p[1][0]), o); o = M32(vf[1][1], asbf(p[1][1]), o);
    SBAR();
    FA(21);
    LANE();
    const float rl = __builtin_amdgcn_rcpf((g & 1) ? ls[1] : ls[0]);
#pragma unroll
    for (int r = 0; r < 16; ++r) o[r] *= rl;
    const int vo = (g & 1) * 1024 + (g >> 1) * 256 + (lane & 15) * 16, so = ((4 * m + tl) * 32 + 2 * hp + hl) * 2048;
    __builtin_amdgcn_raw_buffer_store_b128(pack8(o, 0), ro, vo, so, 16);
    __builtin_amdgcn_raw_buffer_store_b128(pack8(o, 1), ro, vo + 512, so, 16);
  }
  FA(22);
  FG(0);
  PRIO(0);
  LANE();
  gu64* of = (gu64*)(ws + WS_OF);
  gu64* fe = of + (m2 * 4 + tl) * 32 + ((8 * hl + (lane & 7) + r4) & 15);
  unsigned long long seen;
  if (!hl) {
    BAR();
    FG(1);
    ISSUE_WO(0, 1, L_WO);
    if (NA1 > 1) ISSUE_WO(2, 1, L_WO + 32 * KB);
    if (NA1 > 2) ISSUE_WO(1, 1, L_WO + 16 * KB);
    if (NA1 > 3) ISSUE_WO(3, 1, 0);
    FG(2);
    WAIT_V(4 * NA1);
  } else {
    WAIT_V(0);
  }
  if (lane == 0) __hip_atomic_store(of + ((m * 4 + tl) * 2 + hl) * 16 + hp, tag, RLX_AGENT);
  FG(3);
  FSOUT(23);
  STAMP(11);
  if (!hl) {
    if (NA1 <= 1) ISSUE_WO(2, 1, L_WO + 32 * KB);
    if (NA1 <= 2) ISSUE_WO(1, 1, L_WO + 16 * KB);
    if (NA1 <= 3) ISSUE_WO(3, 1, 0);
  }
  {
    FG(4);
    seen = __hip_atomic_load(fe, RLX_AGENT);
    for (unsigned spins = 0; !__all(seen == tag) && spins < SPIN_CAP; ++spins) seen = __hip_atomic_load(fe, RLX_AGENT);
    FG(5);
    STAMP(12);
    bf16x8 a[16];
    const int vl = lane * 16, so = (4 * m2 + tl) * 65536;
    const int sa0 = so + ((8 * hl + r4) & 15) * 4096, sa1 = so + ((8 * hl + 4 + r4) & 15) * 4096;
    auto loadA = [&](int odd) {
#pragma unroll
      for (int i = 0; i < 16; ++i)
        a[i] = asbf(__builtin_amdgcn_raw_buffer_load_b128(ro, vl + (i & 1) * 1024 + odd * 2048, (i < 8 ? sa0 : sa1) + ((i >> 1) & 3) * 4096, 16));
    };
    loadA(0);
    seen = __hip_atomic_load(fe + 16, RLX_AGENT);
    SBAR();
    FG(6);
    const int sw = 2 * (g & 1) + (q4 >> 1);
    const int bw = (16 * (g >> 1) + 4 * (g & 1) + q4) * 128 + 8 * pp + (sw << 5);
    LAS const char* pb[4];
    f32x4 c[2][4];
    s16x4 bl[2][4], bh[2][4];
#pragma unroll
    for (int j = 0; j < 4; ++j) { c[0][j] = f32x4{}; c[1][j] = f32x4{}; pb[j] = L + L_WE + hl * 32768 + (bw ^ (j << 5)); OPQ(pb[j]); }
#define RB(n, j, off) bl[n][j] = ldstr(pb[j] + (off)); bh[n][j] = ldstr(pb[j] + (off) + 1024);
#define MB(b, j, i) c[0][j] = M16(cat(bl[b][j], bh[b][j]), a[2 * (i)], c[0][j]); c[1][j] = M16(cat(bl[b][j], bh[b][j]), a[2 * (i) + 1], c[1][j]); SBAR();
#define HEAD(b, n, i, off) MB(b, 0, i) RB(n, 0, off) SBAR(); MB(b, 1, i) RB(n, 1, off) SBAR(); MB(b, 2, i) RB(n, 2, off) SBAR(); MB(b, 3, i) RB(n, 3, off) SBAR();
#define HEADL(b, i) MB(b, 0, i) MB(b, 1, i) MB(b, 2, i) MB(b, 3, i)
    RB(0, 0, 0) RB(0, 1, 0) RB(0, 2, 0) RB(0, 3, 0) SBAR();
    HEAD(0, 1, 0, 4096) FG(7); HEAD(1, 0, 1, 8192) HEAD(0, 1, 2, 12288) HEAD(1, 0, 3, 16384) FG(8);
    HEAD(0, 1, 4, 20480) HEAD(1, 0, 5, 24576) HEAD(0, 1, 6, 28672) HEADL(1, 7)
    FG(9);
    for (unsigned spins = 0; !__all(seen == tag) && spins < SPIN_CAP; ++spins) seen = __hip_atomic_load(fe + 16, RLX_AGENT);
    FG(10);
    STAMP(14);
    loadA(1);
    SBAR();
    FG(11);
    BAR();
    FG(12);
#pragma unroll
    for (int j = 0; j < 4; ++j) MOVE(pb[j], L_WO - L_WE);
    RB(0, 0, 0) RB(0, 1, 0) RB(0, 2, 0) RB(0, 3, 0) SBAR();
    HEAD(0, 1, 0, 4096) FG(13); HEAD(1, 0, 1, 8192) HEAD(0, 1, 2, 12288)
    {
      const int dlt = hl ? -(L_WO + 32768) : 16384;
#pragma unroll
      for (int j = 0; j < 4; ++j) MOVE(pb[j], dlt);
      SBAR();
    }
    HEAD(1, 0, 3, 0) FG(14);
    HEAD(0, 1, 4, 4096) HEAD(1, 0, 5, 8192) HEAD(0, 1, 6, 12288) HEADL(1, 7)
    FG(15);
    LAS char* xs = L + L_XCH + lane * 16;
#pragma unroll
    for (int j = 0; j < 4; ++j) *(LAS f32x4*)(xs + wid * 4096 + j * 1024) = hl ? c[0][j] : c[1][j];
    WAIT_L0(); FG(16); BAR(); FG(17);
    f32x4 r[4];
#pragma unroll
    for (int j = 0; j < 4; ++j) r[j] = (hl ? c[1][j] : c[0][j]) + *(LAS const f32x4*)(xs + (wid ^ 4) * 4096 + j * 1024);
    char* od = out + (size_t)(128 * m2 + 32 * tl + 16 * hl + (lane & 15)) * 2048 + nb * 128 + (g & 1) * 32 + (g >> 1) * 16;
#pragma unroll
    for (int j = 0; j < 4; j += 2) {
      auto r0 = __builtin_amdgcn_permlane16_swap(pk2(r[j][0], r[j][1]), pk2(r[j + 1][0], r[j + 1][1]), false, false);
      auto r1 = __builtin_amdgcn_permlane16_swap(pk2(r[j][2], r[j][3]), pk2(r[j + 1][2], r[j + 1][3]), false, false);
      __builtin_nontemporal_store((u32x4){r0[0], r1[0], r0[1], r1[1]}, (u32x4*)(od + j * 32));
    }
    FG(18);
  }
  STAMP(16);
  if (bid == 0 && wid == 0) {
    gu64* f = (gu64*)(ws + WS_KVF) + lane * 4;
    for (unsigned spins = 0; spins < SPIN_CAP; ++spins) {
      const bool ok = __hip_atomic_load(f, RLX_AGENT) == tag && __hip_atomic_load(f + 256, RLX_AGENT) == tag &&
                      __hip_atomic_load(f + 512, RLX_AGENT) == tag && __hip_atomic_load(f + 768, RLX_AGENT) == tag;
      if (__all(ok)) break;
      __builtin_amdgcn_s_sleep(1);
    }
    if (lane == 0) __hip_atomic_store((gu64*)(ws + WS_E), tag, RLX_AGENT);
  }
}

__global__ void k_end() { asm volatile("kend_lbl:"); }

extern "C" void kernel_launch(void* const* d_in, const int* in_sizes, int n_in, void* d_out, int out_size, void* d_ws,
                              size_t ws_size, hipStream_t stream) {
  (void)hipFuncSetAttribute((const void*)k_fused, hipFuncAttributeMaxDynamicSharedMemorySize, L_ALL);
  k_fused<<<256, 512, L_ALL, stream>>>((const char*)d_in[0], (const char*)d_in[1], (const char*)d_in[2], (char*)d_ws, (char*)d_out);
}
```

```cpp
#include <hip/hip_runtime.h>
#include <hip/hip_bf16.h>
#include <utility>

typedef short bf16x8 __attribute__((ext_vector_type(8)));
typedef short s16x2 __attribute__((ext_vector_type(2)));
typedef short s16x4 __attribute__((ext_vector_type(4)));
typedef float f32x2 __attribute__((ext_vector_type(2)));
typedef float f32x4 __attribute__((ext_vector_type(4)));
typedef float f32x16 __attribute__((ext_vector_type(16)));
typedef unsigned u32x4 __attribute__((ext_vector_type(4)));
typedef __bf16 bf2_t __attribute__((ext_vector_type(2)));
typedef __amdgpu_buffer_rsrc_t rsrc_t;
#define LAS __attribute__((address_space(3)))
#define GAS __attribute__((address_space(1)))
typedef GAS unsigned long long gu64;
#define DI __device__ __forceinline__
#define M32(a, b, c) __builtin_amdgcn_mfma_f32_32x32x16_bf16((a), (b), (c), 0, 0, 0)
#define M16(a, b, c) __builtin_amdgcn_mfma_f32_16x16x32_bf16((a), (b), (c), 0, 0, 0)
#define SBAR() __builtin_amdgcn_sched_barrier(0)
#define BAR() asm volatile("s_barrier" ::: "memory")
#define WAIT_V(n) asm volatile("s_waitcnt vmcnt(%0)" ::"n"(n) : "memory")
#define WAIT_VL(n) asm volatile("s_waitcnt vmcnt(%0) lgkmcnt(0)" ::"n"(n) : "memory")
#define WAIT_L0() asm volatile("s_waitcnt lgkmcnt(0)" ::: "memory")
#define LGKM0() __builtin_amdgcn_s_waitcnt(0xc07f)
#define VM0() __builtin_amdgcn_s_waitcnt(0x0f70)
#define PRIO(n) __builtin_amdgcn_s_setprio(n)
#define RLX_AGENT __ATOMIC_RELAXED, __HIP_MEMORY_SCOPE_AGENT

#define STAMP(i)
#define TSV(slot)
#define FS(i)
#define FSOUT(n)
#define ROT 2
#define X3P 2
#define FSSET 0
#define CPF 1
#define NA1 2

#define KVS 16
#define OFS 16
constexpr int SEQ = 2048, DM = 1024, KB = 1024;
constexpr int xslot(int t) { return 32 * KB + 16 * KB * (t % 6); }
constexpr int wslot(int t) { return t % 3 == 0 ? 0 : t % 3 == 1 ? 144 * KB : 16 * KB; }
constexpr int pslot(int s) { return s == 0 || s == 4 ? 128 * KB : s == 1 || s == 7 ? 144 * KB : s == 2 ? 16 * KB : s == 3 ? 0 : s == 5 ? 96 * KB : 112 * KB; }
constexpr int L_V = 0, L_K = 48 * KB, L_Q = 128 * KB, L_WE = 96 * KB, L_WO = 48 * KB, L_XCH = 96 * KB, L_ALL = 160 * KB;
constexpr int WROW = 6144, WTILE = 64 * WROW;
constexpr size_t WS_E = 0, WS_GO = 4096, WS_KVF = 65536, WS_OF = 262144, WS_KV = 1u << 20, WS_O = 16u << 20;
constexpr float QSCALE = 0.17677669529663687f * 1.4426950408889634f;
constexpr unsigned SPIN_CAP = 1u << 18;

template <class F, int... I> DI void sfor_(F&& f, std::integer_sequence<int, I...>) { (f(std::integral_constant<int, I>{}), ...); }
template <int N, class F> DI void sfor(F&& f) { sfor_(f, std::make_integer_sequence<int, N>{}); }

DI unsigned pk2(float a, float b) { f32x2 f = {a, b}; return __builtin_bit_cast(unsigned, __builtin_convertvector(f, bf2_t)); }
DI u32x4 pack44(const f32x4& a, const f32x4& b) { return (u32x4){pk2(a[0], a[1]), pk2(a[2], a[3]), pk2(b[0], b[1]), pk2(b[2], b[3])}; }
DI u32x4 pack8(const f32x16& x, int s) {
  return (u32x4){pk2(x[8 * s], x[8 * s + 1]), pk2(x[8 * s + 2], x[8 * s + 3]), pk2(x[8 * s + 4], x[8 * s + 5]), pk2(x[8 * s + 6], x[8 * s + 7])};
}
DI bf16x8 asbf(u32x4 v) { return __builtin_bit_cast(bf16x8, v); }
DI bf16x8 lds128(LAS const char* p) { return *(LAS const bf16x8*)p; }
DI s16x4 ldstr(LAS const char* p) { return __builtin_amdgcn_ds_read_tr16_b64_v4i16((LAS s16x4*)p); }
DI bf16x8 cat(s16x4 a, s16x4 b) { return __builtin_shufflevector(a, b, 0, 1, 2, 3, 4, 5, 6, 7); }
#define DMA4_(AUX)                                                                                             \
  asm volatile("s_add_u32 m0, %0, %1\n\ts_nop 0\n\t"                                                           \
               "buffer_load_dwordx4 %2, %4, %5 offen" AUX " lds\n\t"                                           \
               "buffer_load_dwordx4 %3, %4, %5 offen offset:1024" AUX " lds\n\t"                               \
               "buffer_load_dwordx4 %2, %4, %6 offen offset:2048" AUX " lds\n\t"                               \
               "buffer_load_dwordx4 %3, %4, %6 offen offset:3072" AUX " lds"                                   \
               ::"s"(base), "n"(OFF), "v"(v0), "v"(v1), "s"(r), "s"(s0), "s"(s1) : "memory", "scc")
template <int OFF> DI void dma4(rsrc_t r, unsigned base, int v0, int v1, int s0, int s1) { DMA4_(""); }
template <int OFF> DI void dma4c(rsrc_t r, unsigned base, int v0, int v1, int s0, int s1) { DMA4_(" sc1"); }
DI unsigned long long read_tag(const char* ws) {
  unsigned long long e;
  asm volatile("s_load_dwordx2 %0, %1, 0x0\n\ts_waitcnt lgkmcnt(0)" : "=s"(e) : "s"(ws) : "memory");
  e = e * 0x9E3779B97F4A7C15ull + 0x632BE59BD9B4E019ull;
  asm volatile("" : "+s"(e));
  return e;
}
#define MX3(a, b, c) __builtin_fmaxf(__builtin_fmaxf((a), (b)), (c))
constexpr unsigned long long diag_mask(int r) {
  const int k0 = (r & 3) + 8 * (r >> 3) + 16 * ((r >> 2) & 1);
  return (unsigned long long)(0xffffffffu << k0) | ((unsigned long long)(0xffffffffu << (k0 + 4)) << 32);
}
#define IC(n) std::integral_constant<int, n>{}
#define OPQ(p) asm volatile("" : "+v"(p))
#define MOVE(p, d) do { p += (d); OPQ(p); } while (0)
#define LANE() int lane = threadIdx.x & 63; OPQ(lane); const int g = lane >> 4, q4 = (lane & 15) >> 2, pp = lane & 3; (void)g; (void)q4; (void)pp
#define FA(i) FS(i)
#define FG(i)
#define FP(i)

__global__ __launch_bounds__(512, 2) void k_fused(const char* __restrict__ x, const char* __restrict__ w, const char* __restrict__ wo,
                                                  char* __restrict__ ws, char* __restrict__ out) {
  extern __shared__ __attribute__((aligned(16))) char lds_[];
  LAS char* L = (LAS char*)lds_;
  const unsigned lbase = (unsigned)(size_t)L;
  unsigned long long pc0;
  asm volatile("s_getpc_b64 %0\n\tkstart_lbl:" : "=s"(pc0));
  asm volatile("" ::"s"(x), "s"(w), "s"(wo), "s"(ws), "s"(out));
  const int bid = blockIdx.x, xcd = bid & 7, idx = bid >> 3;
  const int m = (xcd & 1) * 8 + (idx & 7), hp = (xcd >> 1) * 4 + (idx >> 3);
  const int wid = __builtin_amdgcn_readfirstlane(threadIdx.x >> 6), w4 = wid & 3;
  const int kc = idx + (idx >> 3), kg = idx & 4;
  auto ktile = [&](int t) {
    if (!ROT) return t;
    const int a1 = t < 2 || t >= 14 ? 0 : (t + 2) & 12, b1 = t < 2 || t >= 14 ? t : t + 2;
    return ROT == 1 ? (t + 4 * kc) & 15 : (kg ? a1 : t & 12) + (((kg ? b1 : t) + kc) & 3);
  };
  const rsrc_t rw = __builtin_amdgcn_make_buffer_rsrc((void*)(w - 2048), 0, DM * 3 * DM * 2 + 2048, 0x00020000);
  const unsigned lb4 = lbase + w4 * 4096;
  unsigned long long tag;
  int vQ0, vQ1;
  auto issueP = [&](auto sc) {
    constexpr int s = sc;
    dma4<pslot(s)>(rw, lb4, vQ0, vQ1, ktile((10 + 2 * s) & 15) * WTILE + 2048, ktile((11 + 2 * s) & 15) * WTILE);
  };
  const int hl = wid >> 2, tl = w4;
  LAS const char *qs, *vb, *kb;
  u32x4 mk[2];
  bf16x8 ones;
  unsigned long long dm[16];
  auto prep = [&]() {
    LANE();
    qs = L + L_Q + hl * 8192 + tl * 2048 + lane * 16;
    vb = L + L_V + hl * 24576 + tl * 2048 + lane * 16;
    kb = vb + L_K;
    const short dq = (short)((lane & 31) - 4 * (lane >> 5));
    const s16x2 d2 = {dq, dq};
#pragma unroll
    for (int i = 0; i < 8; ++i) {
      const short kk = (short)(((2 * i) & 3) + 8 * (i >> 2) + 16 * ((i >> 1) & 1));
      mk[i >> 2][i & 3] = __builtin_bit_cast(unsigned, (s16x2)(((s16x2){(short)(kk - 1), kk} - d2) >> 15));
    }
    const short one = (((lane >> 4) ^ lane) & 1) ? (short)0 : (short)0x3F80;
    ones = (bf16x8){one, one, one, one, one, one, one, one};
    asm volatile("" : "+v"(mk[0]), "+v"(mk[1]), "+v"(ones), "+v"(qs), "+v"(vb), "+v"(kb));
#pragma unroll
    for (int r = 0; r < 16; ++r) { dm[r] = diag_mask(r); asm volatile("" : "+s"(dm[r])); }
  };
  STAMP(0);
  if (__builtin_expect(wid >= 4, 1)) {
    PRIO(2);
    LANE();
    const rsrc_t rx = __builtin_amdgcn_make_buffer_rsrc((void*)x, 0, SEQ * DM * 2, 0x00020000);
    const int vX0 = (m * 128 + 32 * w4 + (lane >> 3)) * 2048 + (((lane & 7) ^ (lane >> 4)) << 4), vX1 = (vX0 ^ 64) + 16384 - 1024;
    auto issueX = [&](auto tc) { constexpr int t = tc; const int s0 = ktile(t) * 128; dma4<xslot(t)>(rx, lb4, vX0, vX1, s0, s0 + 32768 - 2048); };
    issueX(IC(0));
    SBAR();
    const int vA = (16 * w4 + (lane >> 3)) * WROW + hp * 128 + (((lane & 7) ^ (((lane >> 4) & 1) << 1)) << 4);
    const int vB = (vA ^ 64) + 8 * WROW - 1024;
    auto issueW = [&](auto tc) { constexpr int t = tc; const int s0 = ktile(t) * WTILE + 4096; dma4<wslot(t)>(rw, lb4, vA, vB, s0, s0); };
    issueW(IC(0)); issueX(IC(1)); issueW(IC(1));
    SBAR();
    tag = read_tag(ws);
    LAS const char* px[4];
#pragma unroll
    for (int s = 0; s < 4; ++s) { px[s] = L + 32 * KB + (32 * w4 + (lane & 31)) * 128 + (((2 * s + (lane >> 5)) ^ ((lane >> 1) & 7)) << 4); OPQ(px[s]); }
#define XO(t) (16 * KB * ((t) % 6 < 4 ? (t) % 6 : (t) % 6 - 4))
#define MOVEX(d) do { MOVE(px[0], d); MOVE(px[1], d); MOVE(px[2], d); MOVE(px[3], d); } while (0)
    bf16x8 xh[10][4];
    WAIT_V(8); BAR();
    STAMP(1);
    sfor<14>([&](auto tc) {
      constexpr int t = tc;
      issueX(IC(t + 2)); issueW(IC(t + 2));
      if constexpr (t == 13) issueP(IC(0));
      if constexpr (t == 4 || t == 10) MOVEX(64 * KB);
      if constexpr (t == 6) MOVEX(-64 * KB);
      if constexpr (t == 12) { vQ0 = vA ^ (((lane >> 4) & 1) * 0x60); vQ1 = vQ0 + 8 * WROW - 1024; }
      if constexpr (t < 10) {
#pragma unroll
        for (int s = 0; s < 4; ++s) xh[t][s] = lds128(px[s] + XO(t));
      }
      SBAR();
      WAIT_VL(t == 13 ? 12 : 8); TSV(t + 1); BAR();
    });
    issueP(IC(1)); WAIT_V(8); TSV(15); BAR();
    issueP(IC(2));
    bf16x8 xl[3];
    s16x4 fl[6], fh[6];
    f32x16 aq[2];
    aq[0] = f32x16{}; aq[1] = f32x16{}; OPQ(aq[0]); OPQ(aq[1]);
    xl[0] = lds128(px[0] + XO(10)); xl[1] = lds128(px[1] + XO(10));
    LAS const char* pq[2];
    pq[0] = L + 96 * KB + (8 * (g >> 1) + q4) * 128 + (((g & 1) ^ (q4 & 2)) << 5) + 8 * pp;
    pq[1] = L + 96 * KB + (8 * (g >> 1) + q4) * 128 + (((g & 1) ^ (q4 & 2) ^ 2) << 5) + 8 * pp;
    OPQ(pq[0]); OPQ(pq[1]);
#define PO(n) (pslot((n) >> 4) % (96 * KB) + (((n) >> 3) & 1) * 2048 + (((n) >> 1) & 3) * 4096)
    SBAR();
    WAIT_V(8); TSV(16); BAR();
    PRIO(0);
    STAMP(2);
    FP(0);
    auto rdw = [&](auto nc) { constexpr int n = nc; fl[n % 6] = ldstr(pq[n & 1] + PO(n)); fh[n % 6] = ldstr(pq[n & 1] + PO(n) + 512); };
    auto rdx = [&](auto sc) { constexpr int st = sc; xl[st % 3] = lds128(px[st & 3] + XO(10 + (st >> 2))); };
    sfor<5>([&](auto nc) { rdw(nc); SBAR(); });
    sfor<128>([&](auto nc) {
      constexpr int n = nc, u = n >> 3, st = n >> 1, h = n & 1;
      constexpr int nb = (n | 15) + 1, d = nb - n;
      constexpr bool cut = nb <= 80;
      constexpr int r0 = !cut || d > 5 ? n + 5 : d == 3 ? nb : d == 2 ? nb + 2 : d == 1 ? nb + 4 : 999;
      constexpr int r1 = cut && (d == 3 || d == 2) ? r0 + 1 : 999;
      aq[h] = M32(cat(fl[n % 6], fh[n % 6]), u < 6 ? xl[st % 3] : xh[u < 6 ? 0 : u - 6][st & 3], aq[h]); SBAR();
      if constexpr (r0 < 128) rdw(IC(r0));
      if constexpr (r1 < 128) rdw(IC(r1));
      if constexpr (n == 11) MOVEX(-64 * KB);
      if constexpr (n == 27 || n == 59) { MOVE(pq[0], n == 27 ? -96 * KB : 96 * KB); MOVE(pq[1], n == 27 ? -96 * KB : 96 * KB); }
      if constexpr (cut && d == 4) {
        SBAR();
        if constexpr (nb == 16) WAIT_V(4);
        if constexpr (nb == 32) WAIT_V(0);
        LGKM0(); SBAR(); FP(2 * (nb / 16) - 1); BAR(); FP(2 * (nb / 16)); SBAR();
      }
      if constexpr (h == 0 && st + 2 < 24) rdx(IC(st + 2));
      SBAR();
    });
    FP(11);
    STAMP(3);
    LAS char* dq = L + L_Q + w4 * 2048 + lane * 16;
#pragma unroll
    for (int h = 0; h < 2; ++h) {
      aq[h] *= QSCALE;
#pragma unroll
      for (int c = 0; c < 2; ++c)
        *(LAS u32x4*)(dq + h * 8192 + c * 1024) = (u32x4){pk2(aq[h][4 * c], aq[h][4 * c + 1]), pk2(aq[h][4 * c + 2], aq[h][4 * c + 3]),
                                                          pk2(aq[h][8 + 4 * c], aq[h][9 + 4 * c]), pk2(aq[h][10 + 4 * c], aq[h][11 + 4 * c])};
    }
    WAIT_L0();
    FP(12);
  } else {
    LANE();
    const int wm = wid & 1, wn = wid >> 1;
    unsigned pf = 0;
    if (wid == 0) {
      const unsigned o = (unsigned)(lane * 32 + idx) * 128u;
      unsigned long long keep;
      asm volatile("v_cmp_gt_u32 vcc, kend_lbl - kstart_lbl, %3\n\ts_and_saveexec_b64 %1, vcc\n\t"
                   "global_load_dword %0, %3, %2\n\ts_mov_b64 exec, %1"
                   : "+v"(pf), "=&s"(keep) : "s"(pc0), "v"(o) : "vcc", "scc", "memory");
    }
    tag = read_tag(ws);
    const int ax0 = (64 * wm + (lane & 15)) * 128 + ((g ^ ((lane >> 1) & 7)) << 4), ax1 = ax0 ^ 64;
    const int aw0 = (g >> 1) * 4096 + (8 * (g & 1) + q4) * 128 + (((2 * wn) ^ ((q4 >> 1) & 1) ^ ((g & 1) << 1)) << 5) + 8 * pp, aw1 = aw0 ^ 32;
    vQ0 = (16 * w4 + (lane >> 3)) * WROW + hp * 128 + (((lane & 7) ^ (((lane >> 4) & 1) << 2)) << 4); vQ1 = vQ0 + 8 * WROW - 1024;
    int ok = (g >> 1) * 1024 + (16 * ((lane >> 3) & 1) + (lane & 7) + 32 * (g & 1)) * 16, ov = (g >> 1) * 1024 + ((lane & 15) + 32 * (g & 1)) * 16;
    asm volatile("" : "+v"(vQ0), "+v"(vQ1), "+v"(ok), "+v"(ov));
    f32x4 acc[4][4];
#pragma unroll
    for (int i = 0; i < 4; ++i)
#pragma unroll
      for (int j = 0; j < 4; ++j) { acc[i][j] = f32x4{}; OPQ(acc[i][j]); }
    bf16x8 xf[2][4];
    s16x4 wl[2][4], wh[2][4];
#define WF(b, j) cat(wl[b][j], wh[b][j])
#define RX(n, i) xf[n][i] = lds128(px + (i) * 2048)
#define RL(n, j, s) wl[n][j] = ldstr(((j) & 1 ? pw1 : pw0) + ((j) >> 1) * 2048 + (s) * 8192)
#define RH(n, j, s) wh[n][j] = ldstr(((j) & 1 ? pw1 : pw0) + ((j) >> 1) * 2048 + (s) * 8192 + 512)
#define MM(b, i, j) acc[i][j] = ((j) < 2) ? M16(WF(b, j), xf[b][i], acc[i][j]) : M16(xf[b][i], WF(b, j), acc[i][j])
#define G(mm, rd) mm; SBAR(); rd; SBAR();
#define STEP1(b, n, s)                                                                                        \
  G(MM(b, 0, 0), RX(n, 0)) G(MM(b, 1, 0), RL(n, 0, s)) G(MM(b, 2, 0), RH(n, 0, s)) G(MM(b, 3, 0), RX(n, 1))   \
  G(MM(b, 0, 1), RX(n, 2)) G(MM(b, 1, 1), RX(n, 3)) G(MM(b, 2, 1), RL(n, 1, s)) G(MM(b, 3, 1), RH(n, 1, s))   \
  G(MM(b, 0, 2), RL(n, 2, s)) G(MM(b, 1, 2), RH(n, 2, s)) G(MM(b, 2, 2), RL(n, 3, s)) G(MM(b, 3, 2), RH(n, 3, s))
#define TAIL1(b) MM(b, 0, 3); MM(b, 1, 3); MM(b, 2, 3); MM(b, 3, 3); SBAR(); LGKM0(); SBAR();
    int xs = xslot(0), wsl = wslot(0);
    LAS const char* px = L + xs + ax0;
    LAS const char* pw0 = L + wsl + aw0;
    LAS const char* pw1 = L + wsl + aw1;
    BAR();
    STAMP(1);
    RX(0, 0); RL(0, 0, 0); RH(0, 0, 0); RX(0, 1); RX(0, 2); RX(0, 3); RL(0, 1, 0); RH(0, 1, 0); RL(0, 2, 0); RH(0, 2, 0); RL(0, 3, 0); RH(0, 3, 0);
    px = L + xs + ax1;
    LGKM0(); SBAR();
#pragma unroll 1
    for (int t = 0; t < 15; ++t) {
      STEP1(0, 1, 1)
      xs += 16 * KB; if (xs == 128 * KB) xs = 32 * KB;
      wsl = wsl == 0 ? 144 * KB : wsl == 16 * KB ? 0 : 16 * KB;
      px = L + xs + ax0; pw0 = L + wsl + aw0; pw1 = L + wsl + aw1;
      TAIL1(0)
      asm volatile("s_waitcnt vmcnt(0)" ::"v"(pf) : "memory");
      TSV(t + 1); BAR(); SBAR();
      STEP1(1, 0, 0)
      px = L + xs + ax1;
      TAIL1(1)
    }
    STEP1(0, 1, 1) TAIL1(0)
    TSV(16); BAR(); SBAR();
    PRIO(2);
    STAMP(2);
    FP(0);
    MM(1, 0, 0); MM(1, 0, 1); MM(1, 1, 0); MM(1, 1, 1); SBAR();
    issueP(IC(3)); SBAR();
    MM(1, 2, 0); MM(1, 2, 1); MM(1, 3, 0); MM(1, 3, 1);
    MM(1, 0, 2); MM(1, 1, 2); MM(1, 0, 3); MM(1, 1, 3); MM(1, 2, 2); MM(1, 3, 2); MM(1, 2, 3); MM(1, 3, 3);
    const rsrc_t rkv = __builtin_amdgcn_make_buffer_rsrc((void*)(ws + WS_KV), 0, 8u << 20, 0x00020000);
    const int dg = ((2 * hp + wn) * 64 + 4 * m + 2 * wm) * 4096;
    u32x4 kp[4], vp[4];
#pragma unroll
    for (int i = 0; i < 4; ++i) {
      kp[i] = pack44(acc[i][0], acc[i][1]);
      __builtin_amdgcn_raw_buffer_store_b128(kp[i], rkv, ok + (i & 1) * 128, dg + (i >> 1) * 4096, 16);
    }
#pragma unroll
    for (int i = 0; i < 4; ++i) {
      vp[i] = pack44(acc[(i >> 1) * 2][2 + (i & 1)], acc[(i >> 1) * 2 + 1][2 + (i & 1)]);
      __builtin_amdgcn_raw_buffer_store_b128(vp[i], rkv, ov + 2048 + (i & 1) * 256, dg + (i >> 1) * 4096, 16);
    }
    SBAR();
    FP(1); BAR(); FP(2);
    issueP(IC(4)); issueP(IC(5)); issueP(IC(6));
    FP(3); BAR(); FP(4);
    issueP(IC(7));
    prep();
    WAIT_V(24); FP(5); BAR(); FP(6);
    LAS char* dl = L + wn * 24576 + (8 + 2 * wm) * 2048;
#pragma unroll
    for (int i = 0; i < 4; ++i) *(LAS u32x4*)(dl + L_K + ok + (i >> 1) * 2048 + (i & 1) * 128) = kp[i];
    gu64* kvf = (gu64*)(ws + WS_KVF) + hp * 64 * KVS;
    const int mb = max(m - 2 + wm, 0);
    gu64* fsrc = kvf + (mb * 4 + wn * 2 + (lane & 1)) * KVS;
    WAIT_V(12);
    if (lane < (wid ? 1 : 2)) __hip_atomic_store(lane ? (gu64*)(ws + WS_GO) + hp * 16 + m : kvf + (m * 4 + wid) * KVS, tag, RLX_AGENT);
    FP(7); BAR(); FP(8);
    unsigned long long seen = tag;
    if (lane < 2) seen = __hip_atomic_load(fsrc, RLX_AGENT);
#pragma unroll
    for (int i = 0; i < 4; ++i) *(LAS u32x4*)(dl + L_V + ov + (i >> 1) * 2048 + (i & 1) * 256) = vp[i];
    WAIT_V(2); FP(9); BAR(); FP(10);
    for (unsigned spins = 0; !__all(seen == tag) && spins < SPIN_CAP; ++spins)
      if (lane < 2) seen = __hip_atomic_load(fsrc, RLX_AGENT);
    VM0();
    FP(11);
    STAMP(5);
    const int src = ((2 * hp + wn) * 64 + 4 * mb) * 4096, vl = lane * 16;
    const unsigned dr = lbase + wn * 24576 + wm * 8192;
    dma4c<L_K>(rkv, dr, vl, vl, src, src + 2048); dma4c<L_K + 4096>(rkv, dr, vl, vl, src + 8192, src + 10240);
    dma4c<L_V>(rkv, dr, vl, vl, src + 2048, src + 4096); dma4c<L_V + 4096>(rkv, dr, vl, vl, src + 10240, src + 12288);
    WAIT_VL(8);
    FP(12);
  }
  STAMP(6);
  BAR();
  STAMP(7);
  FA(0);
  if (hl) prep();
  const int m2 = 4 * (xcd & 3) + (idx & 3), nb = 8 * (xcd >> 2) + (idx >> 2), r4 = 4 * (idx & 3);
  const rsrc_t rwo = __builtin_amdgcn_make_buffer_rsrc((void*)wo, 0, DM * DM * 2, 0x00020000);
  const rsrc_t ro = __builtin_amdgcn_make_buffer_rsrc((void*)(ws + WS_O), 0, 4u << 20, 0x00020000);
#define ISSUE_WO(c, odd, dst) do { LANE(); const int v0_ = (lane >> 3) * 2048 + nb * 128 + (((lane & 7) ^ (((lane >> 4) & 3) << 1)) << 4); \
    const int s_ = (2 * ((4 * (c) + w4 + r4) & 15) + (odd)) * 65536; dma4<dst>(rwo, lb4, v0_, v0_ + 16384 - 1024, s_, s_ + 32768 - 2048); } while (0)
  {
    SBAR();
    const bf16x8 q0 = lds128(qs);
    bf16x8 kf[9][2];
#define RK(j) kf[j][0] = lds128(kb + (j) * 2048); kf[j][1] = lds128(kb + (j) * 2048 + 1024); SBAR();
    kf[8][0] = lds128(kb + 8 * 2048);
    const bf16x8 q1 = lds128(qs + 1024);
    kf[8][1] = lds128(kb + 8 * 2048 + 1024);
    SBAR();
    RK(0) RK(7)
    const int jmin = 8 - (4 * m + tl);
    const f32x16 zero = f32x16{};
    f32x16 s[2], sd;
    float mx, ma, mb_;
#define SA(d, j, c) d = M32(kf[j][0], q0, c); SBAR();
#define SB(d, j) d = M32(kf[j][1], q1, d); SBAR();
#define MXA(p) ma = MX3(p[0], p[1], p[2]); mb_ = MX3(p[3], p[4], p[5]); ma = MX3(ma, p[6], p[7]); mb_ = MX3(mb_, p[8], p[9]); SBAR();
#define MXB(p, j) ma = MX3(ma, p[10], p[11]); mb_ = MX3(mb_, p[12], p[13]); ma = MX3(ma, p[14], p[15]); \
    { unsigned pn_ = (j) >= jmin ? 0u : 0xff800000u; asm volatile("" : "+s"(pn_)); const float pen_ = __uint_as_float(pn_); \
      mx = MX3(mx, ma + pen_, mb_ + pen_); } SBAR();
#define MERGE(a, b) _Pragma("unroll") for (int r = (a); r < (b); ++r) sd[r] = __builtin_amdgcn_inverse_ballot_w64(dm[r]) ? sd[r] : s[0][r]; SBAR();
    FA(1);
    SA(sd, 8, zero) FA(2); RK(6) SB(sd, 8) RK(5) SA(s[0], 0, zero) RK(4) SB(s[0], 0) RK(3)
    SA(s[1], 7, zero) RK(2)
    if (__builtin_expect(jmin > 0, 0)) {
#pragma unroll
      for (int r = 0; r < 16; ++r) s[0][r] = -INFINITY;
      OPQ(s[0]);
    }
    SBAR();
    FA(3);
    SB(s[1], 7) RK(1) MERGE(0, 16)
    OPQ(sd);
    FA(4);
    if (hl) { ISSUE_WO(0, 0, L_WE); WAIT_L0(); BAR(); ISSUE_WO(2, 0, L_WE + 32 * KB); }
    SBAR();
    FA(5);
    SA(s[0], 6, zero) MXA(sd) SB(s[0], 6)
    ma = MX3(ma, sd[10], sd[11]); mb_ = MX3(mb_, sd[12], sd[13]); ma = MX3(ma, sd[14], sd[15]); mx = __builtin_fmaxf(ma, mb_); SBAR();
    MXA(s[1]) MXB(s[1], 7)
    FA(6);
    SA(s[1], 5, zero) MXA(s[0]) SB(s[1], 5) MXB(s[0], 6)
    SA(s[0], 4, zero) MXA(s[1]) SB(s[0], 4) MXB(s[1], 5)
    OPQ(mx);
    if (hl) ISSUE_WO(1, 0, L_WE + 16 * KB);
    SBAR();
    FA(7);
    SA(s[1], 3, zero) MXA(s[0]) SB(s[1], 3) MXB(s[0], 4)
    SA(s[0], 2, zero) MXA(s[1]) SB(s[0], 2) MXB(s[1], 3)
    OPQ(mx);
    if (hl) ISSUE_WO(3, 0, L_WE + 48 * KB);
    SBAR();
    FA(8);
    SA(s[1], 1, zero) MXA(s[0]) SB(s[1], 1) MXB(s[0], 2)
    FA(9);
    MXA(s[1]) MXB(s[1], 1)
    { auto rr = __builtin_amdgcn_permlane32_swap(__float_as_uint(mx), __float_as_uint(mx), false, false);
      mx = __builtin_fmaxf(__uint_as_float(rr[0]), __uint_as_float(rr[1])); }
    f32x16 nm;
#pragma unroll
    for (int r = 0; r < 16; ++r) nm[r] = -mx;
    OPQ(nm);
    FA(10);
    if (!hl) { WAIT_V(0); FA(11); BAR(); }
    SBAR();
    FA(12);
    bf16x8 vf[2][2], vx0, vx1;
    u32x4 p[2][2], pd[2];
    f32x16 o = zero;
    f32x4 ls = f32x4{};
    float e[16];
    unsigned pc[8];
#define EDGE(j) if (__builtin_expect((j) == jmin - 1, 0)) { _Pragma("unroll") for (int r = 0; r < 16; ++r) nm[r] = -INFINITY; OPQ(nm); } SBAR();
#define MEET(n) if ((n) == X3P) { if (hl) { WAIT_V(0); BAR(); } SBAR(); }
#define EX(b, r) e[r] = __builtin_amdgcn_exp2f(s[b][r]); OPQ(e[r]);
#define CV(b, i) pc[i] = pk2(e[2 * (i)], e[2 * (i) + 1]); if (((i) & 3) == 3) { p[b][(i) >> 2] = (u32x4){pc[(i) & 4], pc[((i) & 4) + 1], pc[((i) & 4) + 2], pc[((i) & 4) + 3]}; OPQ(p[b][(i) >> 2]); }
#define RV(d0, d1, j) d0 = lds128(vb + (j) * 2048); d1 = lds128(vb + (j) * 2048 + 1024);
    RV(vf[0][0], vf[0][1], 8) RV(vx0, vx1, 0)
    EDGE(7)
    SA(s[1], 7, nm)
#pragma unroll
    for (int r = 0; r < 16; ++r) s[0][r] = sd[r] - mx;
    OPQ(s[0]);
    SBAR();
    SB(s[1], 7)
#pragma unroll
    for (int r = 0; r < 16; ++r) { EX(0, r) }
#pragma unroll
    for (int i = 0; i < 8; ++i) { CV(0, i) }
    pd[0] = p[0][0] & mk[0]; pd[1] = p[0][1] & mk[1];
    OPQ(pd[0]); OPQ(pd[1]);
    SBAR();
    MEET(0)
    FA(13);
    EDGE(6)
    SA(s[0], 6, nm) EX(1, 0) EX(1, 1) EX(1, 2) SBAR();
    SB(s[0], 6) EX(1, 3) EX(1, 4) EX(1, 5) SBAR();
    ls = M16(ones, asbf(p[0][0]), ls); SBAR(); EX(1, 6) SBAR();
    ls = M16(ones, asbf(p[0][1]), ls); SBAR(); p[0][0] ^= pd[0]; SBAR();
    o = M32(vf[0][0], asbf(pd[0]), o); SBAR(); p[0][1] ^= pd[1]; EX(1, 7) SBAR();
    o = M32(vf[0][1], asbf(pd[1]), o); SBAR(); EX(1, 8) EX(1, 9) EX(1, 10) RV(vf[1][0], vf[1][1], 7) SBAR();
    o = M32(vx0, asbf(p[0][0]), o); SBAR(); EX(1, 11) EX(1, 12) EX(1, 13) SBAR();
    o = M32(vx1, asbf(p[0][1]), o); SBAR(); EX(1, 14) EX(1, 15)
    CV(1, 0) CV(1, 1) CV(1, 2) CV(1, 3) CV(1, 4) CV(1, 5) CV(1, 6) CV(1, 7) SBAR();
    MEET(1)
#define ITER(n, b, c)                                                                                         \
    EDGE(7 - (n))                                                                                             \
    SA(s[c], 7 - (n), nm) EX(b, 0) EX(b, 1) EX(b, 2) SBAR();                                                  \
    SB(s[c], 7 - (n)) EX(b, 3) EX(b, 4) EX(b, 5) SBAR();                                                      \
    ITAIL(n, b, c)
#define ITAIL(n, b, c)                                                                                        \
    o = M32(vf[c][0], asbf(p[c][0]), o); SBAR(); EX(b, 6) EX(b, 7) EX(b, 8) SBAR();                           \
    o = M32(vf[c][1], asbf(p[c][1]), o); SBAR(); EX(b, 9) EX(b, 10) EX(b, 11) RV(vf[b][0], vf[b][1], 8 - (n)) SBAR(); \
    ls = M16(ones, asbf(p[c][0]), ls); SBAR(); EX(b, 12) EX(b, 13) CV(b, 0) CV(b, 1) CV(b, 2) SBAR();         \
    ls = M16(ones, asbf(p[c][1]), ls); SBAR(); EX(b, 14) EX(b, 15) CV(b, 3) CV(b, 4) CV(b, 5) CV(b, 6) CV(b, 7) SBAR(); \
    MEET(n)
    FA(14); ITER(2, 0, 1) FA(15); ITER(3, 1, 0) FA(16); ITER(4, 0, 1) FA(17); ITER(5, 1, 0) FA(18); ITER(6, 0, 1) FA(19);
    EX(1, 0) EX(1, 1) EX(1, 2) EX(1, 3) EX(1, 4) EX(1, 5) SBAR();
    ITAIL(7, 1, 0)
    FA(20);
    ls = M16(ones, asbf(p[1][0]), ls); ls = M16(ones, asbf(p[1][1]), ls);
    o = M32(vf[1][0], asbf(p[1][0]), o); o = M32(vf[1][1], asbf(p[1][1]), o);
    SBAR();
    FA(21);
    LANE();
    const float rl = __builtin_amdgcn_rcpf((g & 1) ? ls[1] : ls[0]);
#pragma unroll
    for (int r = 0; r < 16; ++r) o[r] *= rl;
    const int vo = (g & 1) * 1024 + (g >> 1) * 256 + (lane & 15) * 16, so = ((4 * m + tl) * 32 + 2 * hp + hl) * 2048;
    __builtin_amdgcn_raw_buffer_store_b128(pack8(o, 0), ro, vo, so, 16);
    __builtin_amdgcn_raw_buffer_store_b128(pack8(o, 1), ro, vo + 512, so, 16);
  }
  FA(22);
  FG(0);
  PRIO(0);
  LANE();
  gu64* of = (gu64*)(ws + WS_OF);
  gu64* fe = of + ((m2 * 4 + tl) * 32 + ((8 * hl + (lane & 7) + r4) & 15)) * OFS;
  unsigned long long seen = tag, go[4];
  const bool mover = bid == 0 && wid == 4;
  auto readgo = [&]() {
#pragma unroll
    for (int i = 0; i < 4; ++i) go[i] = __hip_atomic_load((gu64*)(ws + WS_GO) + lane + 64 * i, RLX_AGENT);
  };
  if (!hl) {
    BAR();
    FG(1);
    ISSUE_WO(0, 1, L_WO);
    if (NA1 > 1) ISSUE_WO(2, 1, L_WO + 32 * KB);
    if (NA1 > 2) ISSUE_WO(1, 1, L_WO + 16 * KB);
    if (NA1 > 3) ISSUE_WO(3, 1, 0);
    FG(2);
    WAIT_V(4 * NA1);
  } else {
    if (mover) readgo();
    WAIT_V(0);
  }
  if (lane == 0) __hip_atomic_store(of + (((m * 4 + tl) * 2 + hl) * 16 + hp) * OFS, tag, RLX_AGENT);
  if (mover) {
    for (unsigned spins = 0; !__all((go[0] == tag) & (go[1] == tag) & (go[2] == tag) & (go[3] == tag)) && spins < SPIN_CAP; ++spins) readgo();
    if (lane == 0) __hip_atomic_store((gu64*)(ws + WS_E), tag, RLX_AGENT);
  }
  FG(3);
  FSOUT(23);
  STAMP(11);
  if (!hl) {
    if (NA1 <= 1) ISSUE_WO(2, 1, L_WO + 32 * KB);
    if (NA1 <= 2) ISSUE_WO(1, 1, L_WO + 16 * KB);
    if (NA1 <= 3) ISSUE_WO(3, 1, 0);
  }
  {
    FG(4);
    if (lane < 8) seen = __hip_atomic_load(fe, RLX_AGENT);
    for (unsigned spins = 0; !__all(seen == tag) && spins < SPIN_CAP; ++spins)
      if (lane < 8) seen = __hip_atomic_load(fe, RLX_AGENT);
    FG(5);
    STAMP(12);
    bf16x8 a[16];
    const int vl = lane * 16, so = (4 * m2 + tl) * 65536;
    const int sa0 = so + ((8 * hl + r4) & 15) * 4096, sa1 = so + ((8 * hl + 4 + r4) & 15) * 4096;
    auto loadA = [&](int odd) {
#pragma unroll
      for (int i = 0; i < 16; ++i)
        a[i] = asbf(__builtin_amdgcn_raw_buffer_load_b128(ro, vl + (i & 1) * 1024 + odd * 2048, (i < 8 ? sa0 : sa1) + ((i >> 1) & 3) * 4096, 16));
    };
    loadA(0);
    if (lane < 8) seen = __hip_atomic_load(fe + 16 * OFS, RLX_AGENT);
    SBAR();
    FG(6);
    const int sw = 2 * (g & 1) + (q4 >> 1);
    const int bw = (16 * (g >> 1) + 4 * (g & 1) + q4) * 128 + 8 * pp + (sw << 5);
    LAS const char* pb[4];
    f32x4 c[2][4];
    s16x4 bl[2][4], bh[2][4];
#pragma unroll
    for (int j = 0; j < 4; ++j) { c[0][j] = f32x4{}; c[1][j] = f32x4{}; pb[j] = L + L_WE + hl * 32768 + (bw ^ (j << 5)); OPQ(pb[j]); }
#define RB(n, j, off) bl[n][j] = ldstr(pb[j] + (off)); bh[n][j] = ldstr(pb[j] + (off) + 1024);
#define MB(b, j, i) c[0][j] = M16(cat(bl[b][j], bh[b][j]), a[2 * (i)], c[0][j]); c[1][j] = M16(cat(bl[b][j], bh[b][j]), a[2 * (i) + 1], c[1][j]); SBAR();
#define HEAD(b, n, i, off) MB(b, 0, i) RB(n, 0, off) SBAR(); MB(b, 1, i) RB(n, 1, off) SBAR(); MB(b, 2, i) RB(n, 2, off) SBAR(); MB(b, 3, i) RB(n, 3, off) SBAR();
#define HEADL(b, i) MB(b, 0, i) MB(b, 1, i) MB(b, 2, i) MB(b, 3, i)
    RB(0, 0, 0) RB(0, 1, 0) RB(0, 2, 0) RB(0, 3, 0) SBAR();
    HEAD(0, 1, 0, 4096) FG(7); HEAD(1, 0, 1, 8192) HEAD(0, 1, 2, 12288) HEAD(1, 0, 3, 16384) FG(8);
    HEAD(0, 1, 4, 20480) HEAD(1, 0, 5, 24576) HEAD(0, 1, 6, 28672) HEADL(1, 7)
    FG(9);
    for (unsigned spins = 0; !__all(seen == tag) && spins < SPIN_CAP; ++spins)
      if (lane < 8) seen = __hip_atomic_load(fe + 16 * OFS, RLX_AGENT);
    FG(10);
    STAMP(14);
    loadA(1);
    SBAR();
    FG(11);
    BAR();
    FG(12);
#pragma unroll
    for (int j = 0; j < 4; ++j) MOVE(pb[j], L_WO - L_WE);
    RB(0, 0, 0) RB(0, 1, 0) RB(0, 2, 0) RB(0, 3, 0) SBAR();
    HEAD(0, 1, 0, 4096) FG(13); HEAD(1, 0, 1, 8192) HEAD(0, 1, 2, 12288)
    {
      const int dlt = hl ? -(L_WO + 32768) : 16384;
#pragma unroll
      for (int j = 0; j < 4; ++j) MOVE(pb[j], dlt);
      SBAR();
    }
    HEAD(1, 0, 3, 0) FG(14);
    HEAD(0, 1, 4, 4096) HEAD(1, 0, 5, 8192) HEAD(0, 1, 6, 12288) HEADL(1, 7)
    FG(15);
    LAS char* xs = L + L_XCH + lane * 16;
#pragma unroll
    for (int j = 0; j < 4; ++j) *(LAS f32x4*)(xs + wid * 4096 + j * 1024) = hl ? c[0][j] : c[1][j];
    WAIT_L0(); FG(16); BAR(); FG(17);
    f32x4 r[4];
#pragma unroll
    for (int j = 0; j < 4; ++j) r[j] = (hl ? c[1][j] : c[0][j]) + *(LAS const f32x4*)(xs + (wid ^ 4) * 4096 + j * 1024);
    char* od = out + (size_t)(128 * m2 + 32 * tl + 16 * hl + (lane & 15)) * 2048 + nb * 128 + (g & 1) * 32 + (g >> 1) * 16;
#pragma unroll
    for (int j = 0; j < 4; j += 2) {
      auto r0 = __builtin_amdgcn_permlane16_swap(pk2(r[j][0], r[j][1]), pk2(r[j + 1][0], r[j + 1][1]), false, false);
      auto r1 = __builtin_amdgcn_permlane16_swap(pk2(r[j][2], r[j][3]), pk2(r[j + 1][2], r[j + 1][3]), false, false);
      *(u32x4*)(od + j * 32) = (u32x4){r0[0], r1[0], r0[1], r1[1]};
    }
    FG(18);
  }
  STAMP(16);
}

__global__ void k_end() { asm volatile("kend_lbl:"); }

extern "C" void kernel_launch(void* const* d_in, const int* in_sizes, int n_in, void* d_out, int out_size, void* d_ws,
                              size_t ws_size, hipStream_t stream) {
  (void)hipFuncSetAttribute((const void*)k_fused, hipFuncAttributeMaxDynamicSharedMemorySize, L_ALL);
  k_fused<<<256, 512, L_ALL, stream>>>((const char*)d_in[0], (const char*)d_in[1], (const char*)d_in[2], (char*)d_ws, (char*)d_out);
}
```
